```python
import torch

def _softmax(x, axis=-1):
    return torch.softmax(x, dim=axis)
def _gelu(x):
    return torch.nn.functional.gelu(x, approximate="tanh")
def _silu(x):
    return torch.nn.functional.silu(x)
def _sigmoid(x):
    return torch.sigmoid(x)
def _layernorm(x, axis=-1, eps=1e-5):
    m = x.mean(dim=axis, keepdim=True)
    v = x.var(dim=axis, keepdim=True, unbiased=False)
    return (x - m) / torch.sqrt(v + eps)
def _scatter_add(tgt, idx, src, axis=0):
    return tgt.clone().index_add_(axis, idx, src)
def _segment_sum(src, seg, n):
    out = torch.zeros((n,) + src.shape[1:], dtype=src.dtype)
    return out.index_add_(0, seg, src)
def _pad(x, pw):
    flat = tuple(v for pair in reversed(pw) for v in pair)
    return torch.nn.functional.pad(x, flat)

kernel_name = 'concat_heads__grouped_kv_mqa__spectral_fnet__prompt'

def setup_inputs():

    _w = (lambda a: (lambda t: t.to(torch.bfloat16) if t.is_floating_point() else t)(torch.as_tensor(a)))
    def _A_setup():
        B, S, D, H = 1, 2048, 1024, 32
        Dh = D // H
        return {
            "x": _w((lambda *s: torch.randn(*s, dtype=torch.bfloat16) * 0.05)(B, S, D)),
            "wq": _w((lambda *s: torch.randn(*s, dtype=torch.bfloat16) * 3.2)(D, H * Dh)),
            "wk": _w((lambda *s: torch.randn(*s, dtype=torch.bfloat16) * 3.2)(D, Dh)),
            "wv": _w((lambda *s: torch.randn(*s, dtype=torch.bfloat16) * 0.4)(D, Dh)),
            "wo": _w((lambda *s: torch.randn(*s, dtype=torch.bfloat16) * 0.4)(H * Dh, D)),
        }
    _A_in = _A_setup()

    def _B_setup():
        B, S, D = 1, 2048, 1024
        ns = torch.arange(S)[:, None] * torch.arange(S)[None, :]
        nd = torch.arange(D)[:, None] * torch.arange(D)[None, :]
        Ws_re = torch.cos(-2.0 * torch.pi * ns / S).to(float)
        Ws_im = torch.sin(-2.0 * torch.pi * ns / S).to(float)
        Wd_re = torch.cos(-2.0 * torch.pi * nd / D).to(float)
        Wd_im = torch.sin(-2.0 * torch.pi * nd / D).to(float)
        return {
            "x": _w((lambda *s: torch.randn(*s, dtype=torch.bfloat16) * 0.05)(B, S, D)),
            "dft_seq_re": _w(Ws_re),
            "dft_seq_im": _w(Ws_im),
            "dft_hid_re": _w(Wd_re),
            "dft_hid_im": _w(Wd_im),
        }
    _B_in = _B_setup()

    return {
        "A_x": _A_in["x"],
        "A_wq": _A_in["wq"],
        "A_wk": _A_in["wk"],
        "A_wv": _A_in["wv"],
        "A_wo": _A_in["wo"],
        "B_x": _B_in["x"],
        "B_dft_seq_re": _B_in["dft_seq_re"],
        "B_dft_seq_im": _B_in["dft_seq_im"],
        "B_dft_hid_re": _B_in["dft_hid_re"],
        "B_dft_hid_im": _B_in["dft_hid_im"],
    }

def reference(A_x, A_wq, A_wk, A_wv, A_wo, B_x, B_dft_seq_re, B_dft_seq_im, B_dft_hid_re, B_dft_hid_im):
    def _A_ref(x, wq, wk, wv, wo):
        B, S, D = x.shape
        H = 32
        Dh = D // H
        q = (lambda x, *d: x.permute(*d))((x @ wq).reshape(B, S, H, Dh), 0, 2, 1, 3)
        k = x @ wk
        v = x @ wv
        scores = torch.einsum("bhqd,bkd->bhqk", q, k) * (Dh ** -0.5)
        p = _softmax(scores, axis=-1)
        o = torch.einsum("bhqk,bkd->bhqd", p, v)
        return (lambda x, *d: x.permute(*d))(o, 0, 2, 1, 3).reshape(B, S, H * Dh) @ wo

    def _B_ref(x, dft_seq_re, dft_seq_im, dft_hid_re, dft_hid_im):
        a = torch.einsum("bsd,ed->bse", x, dft_hid_re)
        b = torch.einsum("bsd,ed->bse", x, dft_hid_im)
        y = torch.einsum("ts,bse->bte", dft_seq_re, a) - torch.einsum("ts,bse->bte", dft_seq_im, b)
        return y

    a = _A_ref(A_x, A_wq, A_wk, A_wv, A_wo)
    b = _B_ref(B_x, B_dft_seq_re, B_dft_seq_im, B_dft_hid_re, B_dft_hid_im)
    return torch.concatenate([a, b], axis=-1)
```

```cpp
#if defined(__HIP_DEVICE_COMPILE__)
#pragma clang attribute push(__attribute__((target("no-packed-fp32-ops"))), apply_to = function)
#endif
#include <hip/hip_runtime.h>
#include <hip/hip_bf16.h>
#include <stdint.h>

typedef unsigned short bf16_t;
typedef short bf16x8 __attribute__((ext_vector_type(8)));
typedef short s16x4 __attribute__((ext_vector_type(4)));
typedef short v4i16_t __attribute__((ext_vector_type(4)));
typedef float f32x16 __attribute__((ext_vector_type(16)));
typedef float f32x4 __attribute__((ext_vector_type(4)));
typedef float f32x2 __attribute__((ext_vector_type(2)));
typedef __bf16 bf16x2_t __attribute__((ext_vector_type(2)));
typedef unsigned u32x4 __attribute__((ext_vector_type(4)));
typedef unsigned u32x2 __attribute__((ext_vector_type(2)));
#define LAS __attribute__((address_space(3)))
#define GAS __attribute__((address_space(1)))
#define DI __device__ __forceinline__
#define TIDX ((int)__builtin_amdgcn_workitem_id_x())
#define BIDX ((int)__builtin_amdgcn_workgroup_id_x())

constexpr int SEQ = 2048, DM = 1024;
constexpr size_t WS_ABT = 0;
constexpr size_t WS_QF = 8u << 20;
constexpr size_t WS_KF = 12u << 20;
constexpr size_t WS_VF = WS_KF + (128u << 10);
constexpr size_t WS_OP = 13u << 20;

#define MFMA32(a, b, c) __builtin_amdgcn_mfma_f32_32x32x16_bf16((a), (b), (c), 0, 0, 0)
#define MFMA16(a, b, c) __builtin_amdgcn_mfma_f32_16x16x32_bf16((a), (b), (c), 0, 0, 0)

DI unsigned cvtpk(float lo, float hi) { f32x2 v = {lo, hi}; bf16x2_t b = __builtin_convertvector(v, bf16x2_t); return __builtin_bit_cast(unsigned, b); }
typedef __amdgpu_buffer_rsrc_t srd_t;
DI srd_t mksrd(const void* p, unsigned bytes) { return __builtin_amdgcn_make_buffer_rsrc((void*)p, (short)0, (int)bytes, 0x00020000); }
DI void dma4(unsigned m0v, srd_t srd, unsigned soff, unsigned v0, unsigned v1, unsigned v2, unsigned v3) {
    asm volatile("s_mov_b32 m0, %0\n\ts_nop 0\n\t"
                 "buffer_load_dwordx4 %3, %1, %2 offen lds\n\t"
                 "buffer_load_dwordx4 %4, %1, %2 offen offset:1024 lds\n\t"
                 "buffer_load_dwordx4 %5, %1, %2 offen offset:2048 lds\n\t"
                 "buffer_load_dwordx4 %6, %1, %2 offen offset:3072 lds"
                 :: "s"(m0v), "s"(srd), "s"(soff), "v"(v0), "v"(v1), "v"(v2), "v"(v3) : "m0", "memory");
}
DI void dma2(unsigned m0v, srd_t srd, unsigned soff, unsigned v0, unsigned v1) {
    asm volatile("s_mov_b32 m0, %0\n\ts_nop 0\n\t"
                 "buffer_load_dwordx4 %3, %1, %2 offen lds\n\t"
                 "buffer_load_dwordx4 %4, %1, %2 offen offset:1024 lds"
                 :: "s"(m0v), "s"(srd), "s"(soff), "v"(v0), "v"(v1) : "m0", "memory");
}
DI void dma1(unsigned m0v, srd_t srd, unsigned soff, unsigned v0) {
    asm volatile("s_mov_b32 m0, %0\n\ts_nop 0\n\t"
                 "buffer_load_dwordx4 %3, %1, %2 offen lds"
                 :: "s"(m0v), "s"(srd), "s"(soff), "v"(v0) : "m0", "memory");
}
DI s16x4 trread(const LAS char* p) { return __builtin_bit_cast(s16x4, __builtin_amdgcn_ds_read_tr16_b64_v4i16((LAS v4i16_t*)p)); }
template <int N> DI void wait_bar() { asm volatile("s_waitcnt vmcnt(%0) lgkmcnt(0)\n\ts_barrier" ::"n"(N) : "memory"); }
DI u32x4 pack8(const f32x16& x, int s, float sc) {
    u32x4 p;
    p[0] = cvtpk(x[8 * s + 0] * sc, x[8 * s + 1] * sc); p[1] = cvtpk(x[8 * s + 2] * sc, x[8 * s + 3] * sc);
    p[2] = cvtpk(x[8 * s + 4] * sc, x[8 * s + 5] * sc); p[3] = cvtpk(x[8 * s + 6] * sc, x[8 * s + 7] * sc);
    return p;
}

template <int BN, int NST, int NMODE, bool PERMK, class FM, class FN, class Epi>
DI void gemm_tile(LAS char* lds, int nkt, unsigned ldM, unsigned ldN, FM mdma, FN ndma, Epi epi) {
    const int tid = TIDX, lane = tid & 63;
    const int wid = __builtin_amdgcn_readfirstlane(tid >> 6);
    const int wm = wid >> 1, wn = wid & 1, h = lane >> 5, l31 = lane & 31;
    constexpr int NI = BN / 64, STAGE = 16384 + ((NMODE == 0) ? BN * 128 : (NMODE == 1 ? 16384 : 8192));
    constexpr int NDM = 4, NDN = (NMODE == 0) ? BN / 32 : (NMODE == 1 ? 4 : 2), ND = NDM + NDN;
    const unsigned lds0 = (unsigned)(uintptr_t)lds;
    unsigned voffM[NDM], voffN[NDN];
#pragma unroll
    for (int i = 0; i < NDM; ++i) { const int row = 32 * wid + 8 * i + (lane >> 3), ch = (lane & 7) ^ ((row >> 1) & 7); voffM[i] = row * ldM + ch * 16 - i * 1024; }
#pragma unroll
    for (int i = 0; i < NDN; ++i) {
        if constexpr (NMODE == 0) { const int row = (wid * NDN + i) * 8 + (lane >> 3), ch = (lane & 7) ^ ((row >> 1) & 7); voffN[i] = row * ldN + ch * 16 - i * 1024; }
        else if constexpr (NMODE == 1) {
            const int k = 4 * (4 * wid + i) + (lane >> 4), c = lane & 15, ch = c ^ (((k & 3) << 2) | ((k >> 2) & 3));
            const int kk = PERMK ? ((k & ~12) | ((k & 4) << 1) | ((k & 8) >> 1)) : k;
            voffN[i] = kk * ldN + ch * 16 - i * 1024;
        } else voffN[i] = wid * 1024 + lane * 16;
    }
    auto issue = [&](int kt) {
        const unsigned st = lds0 + (kt % NST) * STAGE;
        { srd_t srd; unsigned so; mdma(kt, srd, so); dma4(st + wid * 4096, srd, so, voffM[0], voffM[1], voffM[2], voffM[3]); }
        if constexpr (NMODE == 2) {
            srd_t srd; unsigned so;
            ndma(kt, 0, srd, so); dma1(st + 16384 + wid * 1024, srd, so, voffN[0]);
            ndma(kt, 1, srd, so); dma1(st + 16384 + 4096 + wid * 1024, srd, so, voffN[1]);
        } else {
            srd_t srd; unsigned so; ndma(kt, srd, so);
            if constexpr (NDN == 4) dma4(st + 16384 + wid * 4096, srd, so, voffN[0], voffN[1], voffN[2], voffN[3]);
            else dma2(st + 16384 + wid * 2048, srd, so, voffN[0], voffN[1]);
        }
    };
    const int sw = (lane >> 1) & 7;
    unsigned moff[4], noff[4];
#pragma unroll
    for (int ks = 0; ks < 4; ++ks) {
        moff[ks] = (wm * 64 + l31) * 128 + (((2 * ks + h) ^ sw) << 4);
        noff[ks] = 16384 + (wn * (BN / 2) + l31) * 128 + (((2 * ks + h) ^ sw) << 4);
    }
    unsigned troff[2][2];
    if constexpr (NMODE != 0) {
        const int blk = (lane >> 4) & 1, q = (lane & 15) >> 2, p = lane & 3;
#pragma unroll
        for (int ni = 0; ni < 2; ++ni)
#pragma unroll
            for (int t = 0; t < 2; ++t) {
                const int row = 8 * h + 4 * t + q;
                if constexpr (NMODE == 1) {
                    const int c = wn * 2 + ni, ch = 4 * c + 2 * blk + (p >> 1);
                    troff[ni][t] = 16384 + 256 * row + 16 * (ch ^ (((row & 3) << 2) | ((row >> 2) & 3))) + 8 * (p & 1);
                } else troff[ni][t] = 16384 + ni * 4096 + row * 64 + blk * 32 + p * 8;
            }
    }
    f32x16 acc[2][NI];
#pragma unroll
    for (int mi = 0; mi < 2; ++mi)
#pragma unroll
        for (int ni = 0; ni < NI; ++ni) acc[mi][ni] = f32x16{};
    const bool active = (NMODE != 2) || (wn == 0);
    auto compute = [&](int kt) {
        const LAS char* st = lds + (kt % NST) * STAGE;
        if (active) {
#pragma unroll
            for (int ks = 0; ks < 4; ++ks) {
                bf16x8 mf[2], nf[NI];
#pragma unroll
                for (int mi = 0; mi < 2; ++mi) mf[mi] = *(const LAS bf16x8*)(st + moff[ks] + mi * 4096);
#pragma unroll
                for (int ni = 0; ni < NI; ++ni) {
                    if constexpr (NMODE == 0) nf[ni] = *(const LAS bf16x8*)(st + noff[ks] + ni * 4096);
                    else { constexpr int KSTR = (NMODE == 1) ? 4096 : 1024;
                           const s16x4 lo = trread(st + troff[ni][0] + ks * KSTR), hi = trread(st + troff[ni][1] + ks * KSTR);
                           nf[ni] = __builtin_shufflevector(lo, hi, 0, 1, 2, 3, 4, 5, 6, 7); }
                }
#pragma unroll
                for (int mi = 0; mi < 2; ++mi)
#pragma unroll
                    for (int ni = 0; ni < NI; ++ni) {
                        if (NMODE == 2 && ni == 1) acc[mi][ni] = MFMA32(mf[mi], nf[ni], acc[mi][ni]);
                        else acc[mi][ni] = MFMA32(nf[ni], mf[mi], acc[mi][ni]);
                    }
            }
        }
    };
#pragma unroll
    for (int s = 0; s < NST - 1; ++s) issue(s);
    int kt = 0;
#pragma unroll 1
    for (; kt + NST - 1 < nkt; ++kt) {
        wait_bar<(NST - 2) * ND>();
        issue(kt + NST - 1);
        compute(kt);
    }
#pragma unroll 1
    for (; kt < nkt; ++kt) {
        wait_bar<0>();
        compute(kt);
    }
    if (active) epi(acc, wm, wn, lane);
}

template <int NI> DI void store_rowmajor(f32x16 (&acc)[2][NI], bf16_t* base, unsigned ld, float sc, int lane) {
    const int l31 = lane & 31, h = lane >> 5;
#pragma unroll
    for (int mi = 0; mi < 2; ++mi)
#pragma unroll
        for (int ni = 0; ni < NI; ++ni)
#pragma unroll
            for (int k = 0; k < 4; k += 2) {
                const f32x16& x = acc[mi][ni];
                unsigned ax = cvtpk(x[4 * k] * sc, x[4 * k + 1] * sc), ay = cvtpk(x[4 * k + 2] * sc, x[4 * k + 3] * sc);
                unsigned bx = cvtpk(x[4 * k + 4] * sc, x[4 * k + 5] * sc), by = cvtpk(x[4 * k + 6] * sc, x[4 * k + 7] * sc);
                auto r0 = __builtin_amdgcn_permlane32_swap(ax, bx, false, false);
                auto r1 = __builtin_amdgcn_permlane32_swap(ay, by, false, false);
                u32x4 w = {r0[0], r1[0], r0[1], r1[1]};
                *(u32x4*)(base + (size_t)(mi * 32 + l31) * ld + ni * 32 + 8 * k + 8 * h) = w;
            }
}

constexpr float QSCALE = 0.17677669529663687f * 1.4426950408889634f;

struct ProjArgs { const bf16_t *xa, *wq, *wk, *wv, *xb, *dr, *di; char* ws; };
constexpr int P_NST = 2;
constexpr int P_LDS = P_NST * 32768;

__global__ __launch_bounds__(256, 2) void proj_kernel(ProjArgs a) {
    extern __shared__ __attribute__((aligned(16))) char smem[];
    LAS char* lds = (LAS char*)smem;
    const int b = BIDX;
    if (b < 256) {
        const int xcd = b & 7, j = b >> 3;
        const int mp = (xcd & 3) * 4 + (j & 3), np = (xcd >> 2) * 8 + (j >> 2);
        const bf16_t* dsrc = (mp < 8) ? a.dr + (size_t)mp * 128 * DM : a.di + (size_t)(mp - 8) * 128 * DM;
        const srd_t sd = mksrd(dsrc, 128 * DM * 2), sx = mksrd(a.xb + (size_t)np * 128 * DM, 128 * DM * 2);
        bf16_t* obase = (bf16_t*)(a.ws + WS_ABT) + (size_t)(mp & 7) * 128 * 4096 + (mp >> 3) * 2048 + np * 128;
        const float sc = (mp < 8) ? 1.f : -1.f;
        gemm_tile<128, P_NST, 0, false>(lds, 16, DM * 2, DM * 2, [&](int kt, srd_t& r, unsigned& so) { r = sd; so = kt * 128; }, [&](int kt, srd_t& r, unsigned& so) { r = sx; so = kt * 128; },
            [&](f32x16 (&acc)[2][2], int wm, int wn, int lane) { store_rowmajor<2>(acc, obase + (size_t)wm * 64 * 4096 + wn * 64, 4096, sc, lane); });
    } else if (b < 384) {
        const int bb = b - 256, xcd = bb & 7, j = bb >> 3;
        const int mp = (xcd & 3) * 4 + (j & 3), np = (xcd >> 2) * 4 + (j >> 2);
        const srd_t sx = mksrd(a.xa + (size_t)mp * 128 * DM, 128 * DM * 2), sw = mksrd(a.wq, DM * DM * 2);
        bf16_t* qf = (bf16_t*)(a.ws + WS_QF);
        gemm_tile<128, P_NST, 1, false>(lds, 16, DM * 2, DM * 2, [&](int kt, srd_t& r, unsigned& so) { r = sx; so = kt * 128; }, [&](int kt, srd_t& r, unsigned& so) { r = sw; so = kt * 64 * DM * 2 + np * 256; },
            [&](f32x16 (&acc)[2][2], int wm, int wn, int lane) {
#pragma unroll
                for (int mi = 0; mi < 2; ++mi)
#pragma unroll
                    for (int ni = 0; ni < 2; ++ni) {
                        const int qt = mp * 4 + wm * 2 + mi, head = np * 4 + wn * 2 + ni;
#pragma unroll
                        for (int s = 0; s < 2; ++s) *(u32x4*)(qf + ((size_t)(head * 64 + qt) * 2 + s) * 512 + lane * 8) = pack8(acc[mi][ni], s, QSCALE);
                    }
            });
    } else {
        const int mp = b - 384;
        const srd_t sx = mksrd(a.xa + (size_t)mp * 128 * DM, 128 * DM * 2), sk = mksrd(a.wk, DM * 64), sv = mksrd(a.wv, DM * 64);
        bf16_t* kf = (bf16_t*)(a.ws + WS_KF); bf16_t* vf = (bf16_t*)(a.ws + WS_VF);
        gemm_tile<128, P_NST, 2, false>(lds, 16, DM * 2, 64, [&](int kt, srd_t& r, unsigned& so) { r = sx; so = kt * 128; }, [&](int kt, int which, srd_t& r, unsigned& so) { r = which ? sv : sk; so = kt * 4096; },
            [&](f32x16 (&acc)[2][2], int wm, int wn, int lane) {
#pragma unroll
                for (int mi = 0; mi < 2; ++mi) {
                    const int tile = mp * 4 + wm * 2 + mi;
#pragma unroll
                    for (int s = 0; s < 2; ++s) {
                        *(u32x4*)(kf + ((size_t)tile * 2 + s) * 512 + lane * 8) = pack8(acc[mi][0], s, 1.f);
                        *(u32x4*)(vf + ((size_t)tile * 2 + s) * 512 + lane * 8) = pack8(acc[mi][1], s, 1.f);
                    }
                }
            });
    }
}

constexpr float REF_OFF = 80.f;

__global__ __launch_bounds__(512, 2) void attn_kernel(const char* ws_in, char* ws) {
    __shared__ float xch[8][17][64];
    const int tid = TIDX, lane = tid & 63;
    const int wid = __builtin_amdgcn_readfirstlane(tid >> 6);
    const int g = wid & 3, kh = wid >> 2, h = lane >> 5;
    const int head = BIDX & 31, qb = BIDX >> 5;
    const int qt0 = qb * 8 + g * 2;
    const bf16_t* Qf = (const bf16_t*)(ws_in + WS_QF);
    const bf16x8* Kf = (const bf16x8*)(ws_in + WS_KF) + lane + (size_t)kh * 32 * 128;
    const bf16x8* Vf = (const bf16x8*)(ws_in + WS_VF) + lane + (size_t)kh * 32 * 128;
    bf16x8 q[2][2];
#pragma unroll
    for (int qt = 0; qt < 2; ++qt)
#pragma unroll
        for (int s = 0; s < 2; ++s) q[qt][s] = *(const bf16x8*)(Qf + ((size_t)(head * 64 + qt0 + qt) * 2 + s) * 512 + lane * 8);
    bf16x8 ones[2];
    {
        const bool sel = (((lane >> 2) & 1) == ((lane >> 4) & 1));
#pragma unroll
        for (int qt = 0; qt < 2; ++qt) { const short v = (sel && (lane & 3) == qt) ? (short)0x3F80 : (short)0;
#pragma unroll
            for (int j = 0; j < 8; ++j) ones[qt][j] = v; }
    }
    f32x16 negm[2] = {f32x16{}, f32x16{}}, O[2] = {f32x16{}, f32x16{}};
    float mref[2] = {0.f, 0.f};
    f32x4 lsum = {0.f, 0.f, 0.f, 0.f};
    bf16x8 Kb[2][2], Vb[2][2];
    Kb[0][0] = Kf[0]; Kb[0][1] = Kf[64]; Kb[1][0] = Kf[128]; Kb[1][1] = Kf[128 + 64];
    Vb[0][0] = Vf[0]; Vb[0][1] = Vf[64];
    Vb[1][0] = bf16x8{}; Vb[1][1] = bf16x8{};
    f32x16 SA[2], SB[2]; u32x4 PA[2][2], PB[2][2];
#pragma unroll
    for (int qt = 0; qt < 2; ++qt) { SA[qt] = MFMA32(Kb[0][0], q[qt][0], negm[qt]); SA[qt] = MFMA32(Kb[0][1], q[qt][1], SA[qt]);
#pragma unroll
        for (int s = 0; s < 2; ++s) PB[qt][s] = u32x4{}; }
    { const int t2 = 2; Kb[0][0] = Kf[t2 * 128]; Kb[0][1] = Kf[t2 * 128 + 64]; }

    auto step = [&](int i, f32x16 (&Sc)[2], f32x16 (&Sn)[2], u32x4 (&Pp)[2][2], u32x4 (&Pc)[2][2], bf16x8 (&Kn)[2], bf16x8 (&Vp)[2]) {
#pragma unroll
        for (int qt = 0; qt < 2; ++qt) { Sn[qt] = MFMA32(Kn[0], q[qt][0], negm[qt]); Sn[qt] = MFMA32(Kn[1], q[qt][1], Sn[qt]); }
        { const int tk = (i + 3 < 32) ? i + 3 : 31; Kn[0] = Kf[tk * 128]; Kn[1] = Kf[tk * 128 + 64]; }
#pragma unroll
        for (int qt = 0; qt < 2; ++qt)
#pragma unroll
            for (int s = 0; s < 2; ++s) O[qt] = MFMA32(Vp[s], __builtin_bit_cast(bf16x8, Pp[qt][s]), O[qt]);
        { const int tv = (i + 1 < 32) ? i + 1 : 31; Vp[0] = Vf[tv * 128]; Vp[1] = Vf[tv * 128 + 64]; }
        f32x4 ln = lsum;
#pragma unroll
        for (int qt = 0; qt < 2; ++qt)
#pragma unroll
            for (int s = 0; s < 2; ++s) {
#pragma unroll
                for (int j = 0; j < 4; ++j)
                    Pc[qt][s][j] = cvtpk(__builtin_amdgcn_exp2f(Sc[qt][8 * s + 2 * j]), __builtin_amdgcn_exp2f(Sc[qt][8 * s + 2 * j + 1]));
                ln = MFMA16(ones[qt], __builtin_bit_cast(bf16x8, Pc[qt][s]), ln);
            }
        const bool bad = !(ln[0] <= 1.0f) || !(ln[1] <= 1.0f) || (i == 0);
        if (__builtin_expect(__any(bad), 0)) {
            const bf16x8 k0 = Kf[i * 128], k1 = Kf[i * 128 + 64];
            ln = lsum;
#pragma unroll
            for (int qt = 0; qt < 2; ++qt) {
                f32x16 sr = MFMA32(k0, q[qt][0], f32x16{}); sr = MFMA32(k1, q[qt][1], sr);
                float mx = sr[0];
#pragma unroll
                for (int r = 1; r < 16; ++r) mx = fmaxf(mx, sr[r]);
                { auto rr = __builtin_amdgcn_permlane32_swap(__float_as_uint(mx), __float_as_uint(mx), false, false);
                  mx = fmaxf(__uint_as_float(rr[0]), __uint_as_float(rr[1])); }
                const float mb = mref[qt] + __builtin_amdgcn_logf(lsum[qt]);
                const float mnew = fmaxf(mx, mb) + REF_OFF;
                const float dlt = mref[qt] - mnew;
                const float f = __builtin_amdgcn_exp2f(fminf(dlt, 0.f));
                mref[qt] = mnew;
                ln[qt] *= f;
#pragma unroll
                for (int r = 0; r < 16; ++r) { O[qt][r] *= f; negm[qt][r] = -mnew; Sn[qt][r] += dlt; }
#pragma unroll
                for (int s = 0; s < 2; ++s)
#pragma unroll
                    for (int j = 0; j < 4; ++j)
                        Pc[qt][s][j] = cvtpk(__builtin_amdgcn_exp2f(sr[8 * s + 2 * j] - mnew), __builtin_amdgcn_exp2f(sr[8 * s + 2 * j + 1] - mnew));
            }
#pragma unroll
            for (int qt = 0; qt < 2; ++qt)
#pragma unroll
                for (int s = 0; s < 2; ++s) ln = MFMA16(ones[qt], __builtin_bit_cast(bf16x8, Pc[qt][s]), ln);
        }
        lsum = ln;
    };
    for (int i = 0; i < 32; i += 2) {
        step(i, SA, SB, PB, PA, Kb[1], Vb[1]);
        step(i + 1, SB, SA, PA, PB, Kb[0], Vb[0]);
    }
#pragma unroll
    for (int qt = 0; qt < 2; ++qt)
#pragma unroll
        for (int s = 0; s < 2; ++s) O[qt] = MFMA32(Vb[1][s], __builtin_bit_cast(bf16x8, PB[qt][s]), O[qt]);
    float lse[2];
#pragma unroll
    for (int qt = 0; qt < 2; ++qt) {
        const float inv = 1.0f / lsum[qt];
        lse[qt] = mref[qt] + __builtin_amdgcn_logf(lsum[qt]);
#pragma unroll
        for (int r = 0; r < 16; ++r) O[qt][r] *= inv;
    }
    {
        const int oq = kh ^ 1;
#pragma unroll
        for (int r = 0; r < 16; ++r) xch[wid][r][lane] = oq ? O[1][r] : O[0][r];
        xch[wid][16][lane] = oq ? lse[1] : lse[0];
    }
    __syncthreads();
    {
        const int pw = wid ^ 4;
        const float lo = kh ? lse[1] : lse[0], lp = xch[pw][16][lane];
        const float m = fmaxf(lo, lp);
        float wo = __builtin_amdgcn_exp2f(lo - m), wp = __builtin_amdgcn_exp2f(lp - m);
        const float inv = 1.0f / (wo + wp); wo *= inv; wp *= inv;
        f32x16 r;
#pragma unroll
        for (int i = 0; i < 16; ++i) r[i] = (kh ? O[1][i] : O[0][i]) * wo + xch[pw][i][lane] * wp;
        bf16_t* op = (bf16_t*)(ws + WS_OP) + (size_t)((qt0 + kh) * 32 + (lane & 31)) * DM + head * 32 + 8 * h;
#pragma unroll
        for (int s = 0; s < 2; ++s) *(u32x4*)(op + 16 * s) = pack8(r, s, 1.f);
    }
}

struct OutArgs { const bf16_t *cr, *ci, *wo; const char* ws; bf16_t* out; };
constexpr int O_NST = 3;
constexpr int O_LDS = 2 * 32768 + 16384;

__global__ __launch_bounds__(256, 2) void out_kernel(OutArgs a) {
    extern __shared__ __attribute__((aligned(16))) char smem[];
    LAS char* lds = (LAS char*)smem;
    const int b = BIDX;
    if (b < 256) {
        const int xcd = b & 7, j = b >> 3;
        const int mp = (xcd & 3) * 4 + (j & 3), np = (xcd >> 2) * 8 + (j >> 2);
        const srd_t scr = mksrd(a.cr + (size_t)mp * 128 * SEQ, 128 * SEQ * 2), sci = mksrd(a.ci + (size_t)mp * 128 * SEQ, 128 * SEQ * 2);
        const srd_t sab = mksrd(a.ws + WS_ABT + (size_t)np * 64 * 4096 * 2, 64 * 4096 * 2);
        bf16_t* obase = a.out + (size_t)mp * 128 * 2048 + 1024 + np * 64;
        gemm_tile<64, 3, 0, false>(lds, 64, SEQ * 2, 4096 * 2, [&](int kt, srd_t& r, unsigned& so) { r = kt < 32 ? scr : sci; so = (kt & 31) * 128; }, [&](int kt, srd_t& r, unsigned& so) { r = sab; so = kt * 128; },
            [&](f32x16 (&acc)[2][1], int wm, int wn, int lane) { store_rowmajor<1>(acc, obase + (size_t)wm * 64 * 2048 + wn * 32, 2048, 1.f, lane); });
    } else {
        const int bb = b - 256, xcd = bb & 7, j = bb >> 3;
        const int mp = (xcd & 3) * 4 + (j & 3), np = (xcd >> 2) * 4 + (j >> 2);
        const srd_t so_ = mksrd(a.ws + WS_OP + (size_t)mp * 128 * DM * 2, 128 * DM * 2), sw = mksrd(a.wo, DM * DM * 2);
        bf16_t* obase = a.out + (size_t)mp * 128 * 2048 + np * 128;
        gemm_tile<128, 2, 1, true>(lds, 16, DM * 2, DM * 2, [&](int kt, srd_t& r, unsigned& so) { r = so_; so = kt * 128; }, [&](int kt, srd_t& r, unsigned& so) { r = sw; so = kt * 64 * DM * 2 + np * 256; },
            [&](f32x16 (&acc)[2][2], int wm, int wn, int lane) { store_rowmajor<2>(acc, obase + (size_t)wm * 64 * 2048 + wn * 64, 2048, 1.f, lane); });
    }
}

extern "C" void kernel_launch(void* const* d_in, const int* in_sizes, int n_in, void* d_out, int out_size, void* d_ws, size_t ws_size, hipStream_t stream) {
    const bf16_t* xa = (const bf16_t*)d_in[0]; const bf16_t* wq = (const bf16_t*)d_in[1]; const bf16_t* wk = (const bf16_t*)d_in[2];
    const bf16_t* wv = (const bf16_t*)d_in[3]; const bf16_t* wo = (const bf16_t*)d_in[4]; const bf16_t* xb = (const bf16_t*)d_in[5];
    const bf16_t* cr = (const bf16_t*)d_in[6]; const bf16_t* ci = (const bf16_t*)d_in[7]; const bf16_t* dr = (const bf16_t*)d_in[8]; const bf16_t* di = (const bf16_t*)d_in[9];
    char* ws = (char*)d_ws;
    hipFuncSetAttribute((const void*)proj_kernel, hipFuncAttributeMaxDynamicSharedMemorySize, P_LDS);
    hipFuncSetAttribute((const void*)out_kernel, hipFuncAttributeMaxDynamicSharedMemorySize, O_LDS);
    ProjArgs pa{xa, wq, wk, wv, xb, dr, di, ws};
    proj_kernel<<<400, 256, P_LDS, stream>>>(pa);
    attn_kernel<<<256, 512, 0, stream>>>(ws, ws);
    OutArgs oa{cr, ci, wo, ws, (bf16_t*)d_out};
    out_kernel<<<384, 256, O_LDS, stream>>>(oa);
}

#if defined(__HIP_DEVICE_COMPILE__)
#pragma clang attribute pop
#endif
```

```cpp
#if defined(__HIP_DEVICE_COMPILE__)
#pragma clang attribute push(__attribute__((target("no-packed-fp32-ops"))), apply_to = function)
#endif
#include <hip/hip_runtime.h>
#include <hip/hip_bf16.h>
#include <stdint.h>

typedef unsigned short bf16_t;
typedef short bf16x8 __attribute__((ext_vector_type(8)));
typedef short s16x4 __attribute__((ext_vector_type(4)));
typedef short v4i16_t __attribute__((ext_vector_type(4)));
typedef float f32x16 __attribute__((ext_vector_type(16)));
typedef float f32x4 __attribute__((ext_vector_type(4)));
typedef float f32x2 __attribute__((ext_vector_type(2)));
typedef __bf16 bf16x2_t __attribute__((ext_vector_type(2)));
typedef unsigned u32x4 __attribute__((ext_vector_type(4)));
typedef unsigned u32x2 __attribute__((ext_vector_type(2)));
#define LAS __attribute__((address_space(3)))
#define GAS __attribute__((address_space(1)))
#define DI __device__ __forceinline__
#define TIDX ((int)__builtin_amdgcn_workitem_id_x())
#define BIDX ((int)__builtin_amdgcn_workgroup_id_x())

constexpr int SEQ = 2048, DM = 1024;
constexpr size_t WS_ABT = 0;
constexpr size_t WS_QF = 8u << 20;
constexpr size_t WS_KF = 12u << 20;
constexpr size_t WS_VF = WS_KF + (128u << 10);
constexpr size_t WS_FLAG = 20u << 20;
constexpr size_t WS_OP = 13u << 20;

#define MFMA32(a, b, c) __builtin_amdgcn_mfma_f32_32x32x16_bf16((a), (b), (c), 0, 0, 0)
#define MFMA16(a, b, c) __builtin_amdgcn_mfma_f32_16x16x32_bf16((a), (b), (c), 0, 0, 0)

DI unsigned cvtpk(float lo, float hi) { f32x2 v = {lo, hi}; bf16x2_t b = __builtin_convertvector(v, bf16x2_t); return __builtin_bit_cast(unsigned, b); }
typedef __amdgpu_buffer_rsrc_t srd_t;
DI srd_t mksrd(const void* p, unsigned bytes) { return __builtin_amdgcn_make_buffer_rsrc((void*)p, (short)0, (int)bytes, 0x00020000); }
DI void dma4(unsigned m0v, srd_t srd, unsigned soff, unsigned v0, unsigned v1, unsigned v2, unsigned v3) {
    asm volatile("s_mov_b32 m0, %0\n\ts_nop 0\n\t"
                 "buffer_load_dwordx4 %3, %1, %2 offen lds\n\t"
                 "buffer_load_dwordx4 %4, %1, %2 offen offset:1024 lds\n\t"
                 "buffer_load_dwordx4 %5, %1, %2 offen offset:2048 lds\n\t"
                 "buffer_load_dwordx4 %6, %1, %2 offen offset:3072 lds"
                 :: "s"(m0v), "s"(srd), "s"(soff), "v"(v0), "v"(v1), "v"(v2), "v"(v3) : "m0", "memory");
}
DI void dma4_sc1(unsigned m0v, srd_t srd, unsigned soff, unsigned v0, unsigned v1, unsigned v2, unsigned v3) {
    asm volatile("s_mov_b32 m0, %0\n\ts_nop 0\n\t"
                 "buffer_load_dwordx4 %3, %1, %2 offen sc1 lds\n\t"
                 "buffer_load_dwordx4 %4, %1, %2 offen offset:1024 sc1 lds\n\t"
                 "buffer_load_dwordx4 %5, %1, %2 offen offset:2048 sc1 lds\n\t"
                 "buffer_load_dwordx4 %6, %1, %2 offen offset:3072 sc1 lds"
                 :: "s"(m0v), "s"(srd), "s"(soff), "v"(v0), "v"(v1), "v"(v2), "v"(v3) : "m0", "memory");
}
DI void dma2(unsigned m0v, srd_t srd, unsigned soff, unsigned v0, unsigned v1) {
    asm volatile("s_mov_b32 m0, %0\n\ts_nop 0\n\t"
                 "buffer_load_dwordx4 %3, %1, %2 offen lds\n\t"
                 "buffer_load_dwordx4 %4, %1, %2 offen offset:1024 lds"
                 :: "s"(m0v), "s"(srd), "s"(soff), "v"(v0), "v"(v1) : "m0", "memory");
}
DI void dma1(unsigned m0v, srd_t srd, unsigned soff, unsigned v0) {
    asm volatile("s_mov_b32 m0, %0\n\ts_nop 0\n\t"
                 "buffer_load_dwordx4 %3, %1, %2 offen lds"
                 :: "s"(m0v), "s"(srd), "s"(soff), "v"(v0) : "m0", "memory");
}
DI s16x4 trread(const LAS char* p) { return __builtin_bit_cast(s16x4, __builtin_amdgcn_ds_read_tr16_b64_v4i16((LAS v4i16_t*)p)); }
template <int N> DI void wait_bar() { asm volatile("s_waitcnt vmcnt(%0) lgkmcnt(0)\n\ts_barrier" ::"n"(N) : "memory"); }
DI void st16(void* p, const u32x4& v) { asm volatile("global_store_dwordx4 %0, %1, off\n\ts_nop 1" :: "v"(p), "v"(v) : "memory"); }
DI void st16_sc1(void* p, const u32x4& v) { asm volatile("global_store_dwordx4 %0, %1, off sc1\n\ts_nop 1" :: "v"(p), "v"(v) : "memory"); }
DI u32x4 pack8(const f32x16& x, int s, float sc) {
    u32x4 p;
    p[0] = cvtpk(x[8 * s + 0] * sc, x[8 * s + 1] * sc); p[1] = cvtpk(x[8 * s + 2] * sc, x[8 * s + 3] * sc);
    p[2] = cvtpk(x[8 * s + 4] * sc, x[8 * s + 5] * sc); p[3] = cvtpk(x[8 * s + 6] * sc, x[8 * s + 7] * sc);
    return p;
}

#define SB() __builtin_amdgcn_sched_barrier(0)
template <int N> DI void wait_bar_l() { asm volatile("s_waitcnt lgkmcnt(0)\n\ts_barrier" ::: "memory"); }
DI void touch(unsigned& sink, srd_t srd, unsigned voff, unsigned soff) { asm volatile("buffer_load_dword %0, %1, %2, %3 offen" : "+v"(sink) : "v"(voff), "s"(srd), "s"(soff) : "memory"); }
DI void touch_done(unsigned& sink) { asm volatile("s_waitcnt vmcnt(0)" : "+v"(sink) :: "memory"); }
template <int WM, int WN, int MI, int NI, int NMODE> struct GemmCfg {
    static constexpr int BM = 32 * MI * WM, BN = 32 * NI * WN, CW = WM * WN;
    static constexpr int MBYTES = BM * 128, NBYTES = (NMODE == 2) ? 8192 : BN * 128, STAGE = MBYTES + NBYTES;
    static constexpr int NDM = BM / 32, NDN = (NMODE == 2) ? 2 : BN / 32, ND = NDM + NDN;
};
template <int WM, int WN, int MI, int NI, int NST, int NMODE, int STAGE, class Epi>
DI void gemm_cons(LAS char* lds, int nkt, int wid, int lane, Epi epi) {
    using C = GemmCfg<WM, WN, MI, NI, NMODE>;
    constexpr int BN = C::BN;
    const int wm = wid / WN, wn = wid % WN, h = lane >> 5, l31 = lane & 31, sw = (lane >> 1) & 7;
    unsigned mo[4], no[4];
#pragma unroll
    for (int ks = 0; ks < 4; ++ks) {
        mo[ks] = (wm * 32 * MI + l31) * 128 + (((2 * ks + h) ^ sw) << 4);
        no[ks] = C::MBYTES + (wn * 32 * NI + l31) * 128 + (((2 * ks + h) ^ sw) << 4);
    }
    unsigned troff[2][2];
    if constexpr (NMODE != 0) {
        const int blk = (lane >> 4) & 1, q = (lane & 15) >> 2, p = lane & 3;
#pragma unroll
        for (int ni = 0; ni < NI; ++ni)
#pragma unroll
            for (int t = 0; t < 2; ++t) {
                const int row = 8 * h + 4 * t + q;
                if constexpr (NMODE == 1 && BN == 128) {
                    const int c = wn * 2 + ni, ch = 4 * c + 2 * blk + (p >> 1);
                    troff[ni][t] = C::MBYTES + 256 * row + 16 * (ch ^ (((row & 3) << 2) | ((row >> 2) & 3))) + 8 * (p & 1);
                } else if constexpr (NMODE == 1) {
                    const int ch = 4 * wn + 2 * blk + (p >> 1);
                    troff[ni][t] = C::MBYTES + 128 * row + 16 * (ch ^ (((row >> 1) & 1) << 2)) + 8 * (p & 1);
                } else troff[ni][t] = C::MBYTES + ni * 4096 + row * 64 + blk * 32 + p * 8;
            }
    }
    f32x16 acc[MI][NI];
#pragma unroll
    for (int mi = 0; mi < MI; ++mi)
#pragma unroll
        for (int ni = 0; ni < NI; ++ni) acc[mi][ni] = f32x16{};
    const bool active = (NMODE != 2) || (wn == 0);
    constexpr int NB = (MI * NI > 4) ? 2 : 4;
    bf16x8 FM_[NB][MI], FN_[NB][NI];
    constexpr int KSTR = (NMODE == 1) ? (BN == 128 ? 4096 : 2048) : 1024;
    auto rdM = [&](int b, unsigned st, int ks, int mi) { FM_[b][mi] = *(const LAS bf16x8*)(lds + st + mo[ks] + mi * 4096); };
    auto rdN = [&](int b, unsigned st, int ks, int ni) {
        if constexpr (NMODE == 0) FN_[b][ni] = *(const LAS bf16x8*)(lds + st + no[ks] + ni * 4096);
        else { const s16x4 lo = trread(lds + st + troff[ni][0] + ks * KSTR), hi = trread(lds + st + troff[ni][1] + ks * KSTR);
               FN_[b][ni] = __builtin_shufflevector(lo, hi, 0, 1, 2, 3, 4, 5, 6, 7); }
    };
    auto mm = [&](int b, int mi, int ni) {
        if (NMODE == 2 && ni == 1) acc[mi][ni] = MFMA32(FM_[b][mi], FN_[b][ni], acc[mi][ni]);
        else acc[mi][ni] = MFMA32(FN_[b][ni], FM_[b][mi], acc[mi][ni]);
    };
    auto group = [&](int br, unsigned st, int ks, int bm) {
        if constexpr (MI == 4) {
            mm(bm, 0, 0); SB(); rdN(br, st, ks, 0); rdM(br, st, ks, 0); SB();
            mm(bm, 0, 1); SB(); rdN(br, st, ks, 1); rdM(br, st, ks, 1); SB();
            mm(bm, 1, 0); SB(); rdM(br, st, ks, 2); rdM(br, st, ks, 3); SB();
            mm(bm, 1, 1); SB(); mm(bm, 2, 0); SB(); mm(bm, 2, 1); SB(); mm(bm, 3, 0); SB(); mm(bm, 3, 1); SB();
        } else if constexpr (NI == 2) {
            mm(bm, 0, 0); SB(); rdM(br, st, ks, 0); rdN(br, st, ks, 0); SB();
            mm(bm, 0, 1); SB(); rdN(br, st, ks, 1); rdM(br, st, ks, 1); SB();
            mm(bm, 1, 0); SB(); mm(bm, 1, 1); SB();
        } else {
            mm(bm, 0, 0); SB(); rdM(br, st, ks, 0); rdN(br, st, ks, 0); rdM(br, st, ks, 1); SB();
            mm(bm, 1, 0); SB();
        }
    };
    wait_bar_l<0>();
    if (active) {
#pragma unroll
        for (int ks = 0; ks < NB / 2; ++ks) {
#pragma unroll
            for (int mi = 0; mi < MI; ++mi) rdM(ks, 0, ks, mi);
#pragma unroll
            for (int ni = 0; ni < NI; ++ni) rdN(ks, 0, ks, ni); }
    }
    unsigned st = 0;
#pragma unroll 1
    for (int kt = 0; kt < nkt; ++kt) {
        const unsigned stn = (st + STAGE == NST * STAGE) ? 0 : st + STAGE;
        if constexpr (NB == 4) {
            if (active) { group(2, st, 2, 0); group(3, st, 3, 1); }
            wait_bar_l<0>(); SB();
            if (active) { group(0, stn, 0, 2); group(1, stn, 1, 3); }
        } else {
            if (active) { group(1, st, 1, 0); group(0, st, 2, 1); group(1, st, 3, 0); }
            wait_bar_l<0>(); SB();
            if (active) group(0, stn, 0, 1);
        }
        st = stn;
    }
    wait_bar_l<0>();
    if (active) epi(acc, wm, wn);
}
template <int WM, int WN, int MI, int NI, int NST, int NMODE, bool PERMK, class FM, class FN, class Epi>
DI void gemm_pc(LAS char* lds, int nkt, unsigned ldM, unsigned ldN, FM mdma, FN ndma, Epi epi) {
    using C = GemmCfg<WM, WN, MI, NI, NMODE>;
    constexpr int STAGE = C::STAGE, NDM = C::NDM, NDN = C::NDN, ND = C::ND, BN = C::BN;
    const int tid = TIDX, lane = tid & 63;
    const int wid = __builtin_amdgcn_readfirstlane(tid >> 6);
    const unsigned lds0 = (unsigned)(uintptr_t)lds;
    if (wid >= C::CW) {
        const int pw = wid - C::CW;
        unsigned voffM[NDM], voffN[NDN];
#pragma unroll
        for (int i = 0; i < NDM; ++i) { const int row = (pw * NDM + i) * 8 + (lane >> 3), ch = (lane & 7) ^ ((row >> 1) & 7); voffM[i] = row * ldM + ch * 16 - (i & 3) * 1024; }
#pragma unroll
        for (int i = 0; i < NDN; ++i) {
            if constexpr (NMODE == 0) { const int row = (pw * NDN + i) * 8 + (lane >> 3), ch = (lane & 7) ^ ((row >> 1) & 7); voffN[i] = row * ldN + ch * 16 - i * 1024; }
            else if constexpr (NMODE == 1) {
                int k, ch;
                if constexpr (BN == 128) { k = 4 * (4 * pw + i) + (lane >> 4); ch = (lane & 15) ^ (((k & 3) << 2) | ((k >> 2) & 3)); }
                else { k = 8 * (2 * pw + i) + (lane >> 3); ch = (lane & 7) ^ (((k >> 1) & 1) << 2); }
                const int kk = PERMK ? ((k & ~12) | ((k & 4) << 1) | ((k & 8) >> 1)) : k;
                voffN[i] = kk * ldN + ch * 16 - i * 1024;
            } else voffN[i] = pw * 1024 + lane * 16;
        }
        auto issue = [&](int kt, unsigned slot) {
            const unsigned st = lds0 + slot;
            const bool in = (unsigned)kt < (unsigned)nkt;
            { srd_t srd; unsigned so; mdma(kt, srd, so); so = in ? so : 0x40000000u;
#pragma unroll
              for (int i = 0; i < NDM; i += 4) dma4(st + (pw * NDM + i) * 1024, srd, so, voffM[i], voffM[i + 1], voffM[i + 2], voffM[i + 3]); }
            if constexpr (NMODE == 2) {
                srd_t srd; unsigned so;
                ndma(kt, 0, srd, so); dma1(st + C::MBYTES + pw * 1024, srd, in ? so : 0x40000000u, voffN[0]);
                ndma(kt, 1, srd, so); dma1(st + C::MBYTES + 4096 + pw * 1024, srd, in ? so : 0x40000000u, voffN[1]);
            } else {
                srd_t srd; unsigned so; ndma(kt, srd, so); so = in ? so : 0x40000000u;
                if constexpr (NDN == 4) dma4(st + C::MBYTES + pw * 4096, srd, so, voffN[0], voffN[1], voffN[2], voffN[3]);
                else dma2(st + C::MBYTES + pw * 2048, srd, so, voffN[0], voffN[1]);
            }
        };
#pragma unroll
        for (int s = 0; s < NST; ++s) issue(s, s * STAGE);
        wait_bar<(NST - 1) * ND>();
        unsigned st = 0;
#pragma unroll 1
        for (int kt = 0; kt < nkt; ++kt) {
            wait_bar<(NST - 2) * ND>();
            issue(kt + NST, st);
            st = (st + STAGE == NST * STAGE) ? 0 : st + STAGE;
        }
        wait_bar<0>();
        return;
    }
    gemm_cons<WM, WN, MI, NI, NST, NMODE, STAGE>(lds, nkt, wid, lane, [&](f32x16 (&acc)[MI][NI], int wm, int wn) { epi(acc, wm, wn, lane, lds + wid * (4096 * MI)); });
}

template <int MI, int NI> DI void store_rowmajor(f32x16 (&acc)[MI][NI], bf16_t* base, unsigned ld, float sc, int lane, LAS char* stg) {
    const int l31 = lane & 31, h = lane >> 5;
    constexpr int RB = 64 * NI;
#pragma unroll
    for (int mi = 0; mi < MI; ++mi)
#pragma unroll
        for (int ni = 0; ni < NI; ++ni)
#pragma unroll
            for (int g = 0; g < 4; ++g) {
                const f32x16& x = acc[mi][ni];
                u32x2 w = {cvtpk(x[4 * g] * sc, x[4 * g + 1] * sc), cvtpk(x[4 * g + 2] * sc, x[4 * g + 3] * sc)};
                const int row = mi * 32 + l31, ch = ni * 4 + g;
                const int pch = (NI == 2) ? (ch ^ (row & 7)) : (ch ^ ((row >> 1) & 3));
                *(LAS u32x2*)(stg + row * RB + pch * 16 + h * 8) = w;
            }
    constexpr int CPR = 4 * NI, RPI = 64 / CPR;
#pragma unroll
    for (int i = 0; i < 32 * MI / RPI; ++i) {
        const int row = i * RPI + lane / CPR, ch = lane % CPR;
        const int pch = (NI == 2) ? (ch ^ (row & 7)) : (ch ^ ((row >> 1) & 3));
        const u32x4 v = *(const LAS u32x4*)(stg + row * RB + pch * 16);
        st16(base + (size_t)row * ld + ch * 8, v);
    }
}

constexpr float QSCALE = 0.17677669529663687f * 1.4426950408889634f;

struct ProjArgs { const bf16_t *xa, *wq, *wk, *wv, *xb, *dr, *di; char* ws; };
constexpr int P_NST = 3;
constexpr int P_LDS = P_NST * 49152;

__global__ __launch_bounds__(768, 3) void proj_kernel(ProjArgs a) {
    extern __shared__ __attribute__((aligned(16))) char smem[];
    LAS char* lds = (LAS char*)smem;
    const int b = BIDX;
    if (b < 128) {
        const int xcd = b & 7, j = b >> 3;
        const int mp = (xcd & 3) * 2 + (j & 1), np = (xcd >> 2) * 8 + (j >> 1);
        const bf16_t* dsrc = (mp < 4) ? a.dr + (size_t)mp * 256 * DM : a.di + (size_t)(mp - 4) * 256 * DM;
        const srd_t sd = mksrd(dsrc, 256 * DM * 2), sx = mksrd(a.xb + (size_t)np * 128 * DM, 128 * DM * 2);
        bf16_t* obase = (bf16_t*)(a.ws + WS_ABT) + (size_t)(mp & 3) * 256 * 4096 + (mp >> 2) * 2048 + np * 128;
        const float sc = (mp < 4) ? 1.f : -1.f;
        gemm_pc<4, 2, 2, 2, P_NST, 0, false>(lds, 16, DM * 2, DM * 2, [&](int kt, srd_t& r, unsigned& so) { r = sd; so = kt * 128; }, [&](int kt, srd_t& r, unsigned& so) { r = sx; so = kt * 128; },
            [&](f32x16 (&acc)[2][2], int wm, int wn, int lane, LAS char* stg) { store_rowmajor<2, 2>(acc, obase + (size_t)wm * 64 * 4096 + wn * 64, 4096, sc, lane, stg); });
    } else if (b < 192) {
        const int bb = b - 128, xcd = bb & 7, j = bb >> 3;
        const int mp = (xcd & 3) * 2 + (j & 1), np = (xcd >> 2) * 4 + (j >> 1);
        const srd_t sx = mksrd(a.xa + (size_t)mp * 256 * DM, 256 * DM * 2), sw = mksrd(a.wq, DM * DM * 2);
        bf16_t* qf = (bf16_t*)(a.ws + WS_QF);
        gemm_pc<4, 2, 2, 2, P_NST, 1, false>(lds, 16, DM * 2, DM * 2, [&](int kt, srd_t& r, unsigned& so) { r = sx; so = kt * 128; }, [&](int kt, srd_t& r, unsigned& so) { r = sw; so = kt * 64 * DM * 2 + np * 256; },
            [&](f32x16 (&acc)[2][2], int wm, int wn, int lane, LAS char* stg) {
#pragma unroll
                for (int mi = 0; mi < 2; ++mi)
#pragma unroll
                    for (int ni = 0; ni < 2; ++ni) {
                        const int qt = mp * 8 + wm * 2 + mi, head = np * 4 + wn * 2 + ni;
#pragma unroll
                        for (int s = 0; s < 2; ++s) st16(qf + ((size_t)(head * 64 + qt) * 2 + s) * 512 + lane * 8, pack8(acc[mi][ni], s, QSCALE));
                    }
            });
    } else if (b >= 200) {
        if (TIDX < 8) ((unsigned*)(a.ws + WS_FLAG))[TIDX * 64] = 0u;
    } else {
        const int mp = b - 192;
        const srd_t sx = mksrd(a.xa + (size_t)mp * 256 * DM, 256 * DM * 2), sk = mksrd(a.wk, DM * 64), sv = mksrd(a.wv, DM * 64);
        bf16_t* kf = (bf16_t*)(a.ws + WS_KF); bf16_t* vf = (bf16_t*)(a.ws + WS_VF);
        gemm_pc<4, 2, 2, 2, P_NST, 2, false>(lds, 16, DM * 2, 64, [&](int kt, srd_t& r, unsigned& so) { r = sx; so = kt * 128; }, [&](int kt, int which, srd_t& r, unsigned& so) { r = which ? sv : sk; so = kt * 4096; },
            [&](f32x16 (&acc)[2][2], int wm, int wn, int lane, LAS char* stg) {
#pragma unroll
                for (int mi = 0; mi < 2; ++mi) {
                    const int tile = mp * 8 + wm * 2 + mi;
#pragma unroll
                    for (int s = 0; s < 2; ++s) {
                        st16(kf + ((size_t)tile * 2 + s) * 512 + lane * 8, pack8(acc[mi][0], s, 1.f));
                        st16(vf + ((size_t)tile * 2 + s) * 512 + lane * 8, pack8(acc[mi][1], s, 1.f));
                    }
                }
            });
    }
}

constexpr float REF_OFF = 80.f;
#define PIN(x) asm volatile("" : "+v"(x))
#define EX(x) __builtin_amdgcn_exp2f(x)
#define BC8(x) __builtin_bit_cast(bf16x8, x)
DI bf16x8 bld(srd_t r, unsigned vo, unsigned so) { return __builtin_bit_cast(bf16x8, __builtin_amdgcn_raw_buffer_load_b128(r, vo, so, 0)); }
#define FUSED_LOOP_ASM \
    "s_mov_b32 %[cnt], 16\n\t" \
    ".Lloop_%=:\n\t" \
    "v_mfma_f32_16x16x32_bf16 v[128:131], v[136:139], v[104:107], v[128:131]\n\t" \
    "ds_read_b128 v[216:219], v233 offset:0\n\t" \
    "ds_read_b128 v[208:211], v229 offset:0\n\t" \
    "s_waitcnt lgkmcnt(2)\n\t" \
    "v_mfma_f32_32x32x16_bf16 v[80:95], v[148:151], v[120:123], v[48:63]\n\t" \
    "v_exp_f32 v220, v64\n\t" \
    "v_exp_f32 v221, v65\n\t" \
    "v_exp_f32 v222, v66\n\t" \
    "v_mfma_f32_32x32x16_bf16 v[164:179], v[204:207], v[196:199], v[164:179]\n\t" \
    "v_exp_f32 v223, v67\n\t" \
    "v_exp_f32 v224, v68\n\t" \
    "v_cvt_pk_bf16_f32 v96, v220, v221\n\t" \
    "ds_read_b128 v[212:215], v229 offset:4096\n\t" \
    "v_mfma_f32_32x32x16_bf16 v[80:95], v[152:155], v[124:127], v[80:95]\n\t" \
    "v_exp_f32 v225, v69\n\t" \
    "v_exp_f32 v226, v70\n\t" \
    "v_cvt_pk_bf16_f32 v97, v222, v223\n\t" \
    "ds_read_b128 v[148:151], v236 offset:0\n\t" \
    "v_mfma_f32_32x32x16_bf16 v[180:195], v[204:207], v[200:203], v[180:195]\n\t" \
    "v_exp_f32 v227, v71\n\t" \
    "v_cvt_pk_bf16_f32 v98, v224, v225\n\t" \
    "ds_read_b128 v[152:155], v236 offset:1024\n\t" \
    "v_cvt_pk_bf16_f32 v99, v226, v227\n\t" \
    "v_mfma_f32_16x16x32_bf16 v[128:131], v[136:139], v[108:111], v[128:131]\n\t" \
    "ds_read_b128 v[204:207], v234 offset:0\n\t" \
    "ds_read_b128 v[196:199], v230 offset:0\n\t" \
    "s_waitcnt lgkmcnt(4)\n\t" \
    "v_mfma_f32_32x32x16_bf16 v[16:31], v[156:159], v[104:107], v[16:31]\n\t" \
    "v_exp_f32 v220, v72\n\t" \
    "v_exp_f32 v221, v73\n\t" \
    "v_exp_f32 v222, v74\n\t" \
    "v_mfma_f32_32x32x16_bf16 v[164:179], v[216:219], v[208:211], v[164:179]\n\t" \
    "v_exp_f32 v223, v75\n\t" \
    "v_exp_f32 v224, v76\n\t" \
    "v_cvt_pk_bf16_f32 v100, v220, v221\n\t" \
    "ds_read_b128 v[200:203], v230 offset:4096\n\t" \
    "v_mfma_f32_32x32x16_bf16 v[16:31], v[160:163], v[108:111], v[16:31]\n\t" \
    "v_exp_f32 v225, v77\n\t" \
    "v_exp_f32 v226, v78\n\t" \
    "v_cvt_pk_bf16_f32 v101, v222, v223\n\t" \
    "ds_read_b128 v[156:159], v236 offset:2048\n\t" \
    "v_mfma_f32_32x32x16_bf16 v[180:195], v[216:219], v[212:215], v[180:195]\n\t" \
    "v_exp_f32 v227, v79\n\t" \
    "v_cvt_pk_bf16_f32 v102, v224, v225\n\t" \
    "ds_read_b128 v[160:163], v236 offset:3072\n\t" \
    "v_cvt_pk_bf16_f32 v103, v226, v227\n\t" \
    "v_mfma_f32_16x16x32_bf16 v[128:131], v[132:135], v[96:99], v[128:131]\n\t" \
    "ds_read_b128 v[216:219], v235 offset:0\n\t" \
    "ds_read_b128 v[208:211], v231 offset:0\n\t" \
    "s_waitcnt lgkmcnt(4)\n\t" \
    "v_mfma_f32_32x32x16_bf16 v[64:79], v[148:151], v[112:115], v[32:47]\n\t" \
    "v_exp_f32 v220, v80\n\t" \
    "v_exp_f32 v221, v81\n\t" \
    "v_exp_f32 v222, v82\n\t" \
    "v_mfma_f32_32x32x16_bf16 v[164:179], v[204:207], v[196:199], v[164:179]\n\t" \
    "v_exp_f32 v223, v83\n\t" \
    "v_exp_f32 v224, v84\n\t" \
    "v_cvt_pk_bf16_f32 v104, v220, v221\n\t" \
    "ds_read_b128 v[212:215], v231 offset:4096\n\t" \
    "v_mfma_f32_32x32x16_bf16 v[64:79], v[152:155], v[116:119], v[64:79]\n\t" \
    "v_exp_f32 v225, v85\n\t" \
    "v_exp_f32 v226, v86\n\t" \
    "v_cvt_pk_bf16_f32 v105, v222, v223\n\t" \
    "v_mfma_f32_32x32x16_bf16 v[180:195], v[204:207], v[200:203], v[180:195]\n\t" \
    "v_exp_f32 v227, v87\n\t" \
    "v_cvt_pk_bf16_f32 v106, v224, v225\n\t" \
    "v_cvt_pk_bf16_f32 v107, v226, v227\n\t" \
    "s_waitcnt lgkmcnt(0)\n\t" \
    "s_barrier\n\t" \
    "v_mfma_f32_16x16x32_bf16 v[128:131], v[132:135], v[100:103], v[128:131]\n\t" \
    "ds_read_b128 v[204:207], v232 offset:28672\n\t" \
    "ds_read_b128 v[196:199], v228 offset:28672\n\t" \
    "v_mfma_f32_32x32x16_bf16 v[0:15], v[156:159], v[96:99], v[0:15]\n\t" \
    "v_exp_f32 v220, v88\n\t" \
    "v_exp_f32 v221, v89\n\t" \
    "v_exp_f32 v222, v90\n\t" \
    "v_mfma_f32_32x32x16_bf16 v[164:179], v[216:219], v[208:211], v[164:179]\n\t" \
    "v_exp_f32 v223, v91\n\t" \
    "v_exp_f32 v224, v92\n\t" \
    "v_cvt_pk_bf16_f32 v108, v220, v221\n\t" \
    "ds_read_b128 v[200:203], v228 offset:32768\n\t" \
    "v_mfma_f32_32x32x16_bf16 v[0:15], v[160:163], v[100:103], v[0:15]\n\t" \
    "v_exp_f32 v225, v93\n\t" \
    "v_exp_f32 v226, v94\n\t" \
    "v_cvt_pk_bf16_f32 v109, v222, v223\n\t" \
    "v_mfma_f32_32x32x16_bf16 v[180:195], v[216:219], v[212:215], v[180:195]\n\t" \
    "v_exp_f32 v227, v95\n\t" \
    "v_cvt_pk_bf16_f32 v110, v224, v225\n\t" \
    "v_cvt_pk_bf16_f32 v111, v226, v227\n\t" \
    "v_mfma_f32_16x16x32_bf16 v[128:131], v[136:139], v[104:107], v[128:131]\n\t" \
    "ds_read_b128 v[216:219], v233 offset:28672\n\t" \
    "ds_read_b128 v[208:211], v229 offset:28672\n\t" \
    "s_waitcnt lgkmcnt(2)\n\t" \
    "v_mfma_f32_32x32x16_bf16 v[80:95], v[148:151], v[120:123], v[48:63]\n\t" \
    "v_exp_f32 v220, v64\n\t" \
    "v_exp_f32 v221, v65\n\t" \
    "v_exp_f32 v222, v66\n\t" \
    "v_mfma_f32_32x32x16_bf16 v[164:179], v[204:207], v[196:199], v[164:179]\n\t" \
    "v_exp_f32 v223, v67\n\t" \
    "v_exp_f32 v224, v68\n\t" \
    "v_cvt_pk_bf16_f32 v96, v220, v221\n\t" \
    "ds_read_b128 v[212:215], v229 offset:32768\n\t" \
    "v_mfma_f32_32x32x16_bf16 v[80:95], v[152:155], v[124:127], v[80:95]\n\t" \
    "v_exp_f32 v225, v69\n\t" \
    "v_exp_f32 v226, v70\n\t" \
    "v_cvt_pk_bf16_f32 v97, v222, v223\n\t" \
    "ds_read_b128 v[148:151], v236 offset:28672\n\t" \
    "v_mfma_f32_32x32x16_bf16 v[180:195], v[204:207], v[200:203], v[180:195]\n\t" \
    "v_exp_f32 v227, v71\n\t" \
    "v_cvt_pk_bf16_f32 v98, v224, v225\n\t" \
    "ds_read_b128 v[152:155], v236 offset:29696\n\t" \
    "v_cvt_pk_bf16_f32 v99, v226, v227\n\t" \
    "v_mfma_f32_16x16x32_bf16 v[128:131], v[136:139], v[108:111], v[128:131]\n\t" \
    "ds_read_b128 v[204:207], v234 offset:28672\n\t" \
    "ds_read_b128 v[196:199], v230 offset:28672\n\t" \
    "s_waitcnt lgkmcnt(4)\n\t" \
    "v_mfma_f32_32x32x16_bf16 v[16:31], v[156:159], v[104:107], v[16:31]\n\t" \
    "v_exp_f32 v220, v72\n\t" \
    "v_exp_f32 v221, v73\n\t" \
    "v_exp_f32 v222, v74\n\t" \
    "v_mfma_f32_32x32x16_bf16 v[164:179], v[216:219], v[208:211], v[164:179]\n\t" \
    "v_exp_f32 v223, v75\n\t" \
    "v_exp_f32 v224, v76\n\t" \
    "v_cvt_pk_bf16_f32 v100, v220, v221\n\t" \
    "ds_read_b128 v[200:203], v230 offset:32768\n\t" \
    "v_mfma_f32_32x32x16_bf16 v[16:31], v[160:163], v[108:111], v[16:31]\n\t" \
    "v_exp_f32 v225, v77\n\t" \
    "v_exp_f32 v226, v78\n\t" \
    "v_cvt_pk_bf16_f32 v101, v222, v223\n\t" \
    "ds_read_b128 v[156:159], v236 offset:30720\n\t" \
    "v_mfma_f32_32x32x16_bf16 v[180:195], v[216:219], v[212:215], v[180:195]\n\t" \
    "v_exp_f32 v227, v79\n\t" \
    "v_cvt_pk_bf16_f32 v102, v224, v225\n\t" \
    "ds_read_b128 v[160:163], v236 offset:31744\n\t" \
    "v_cvt_pk_bf16_f32 v103, v226, v227\n\t" \
    "v_mfma_f32_16x16x32_bf16 v[128:131], v[132:135], v[96:99], v[128:131]\n\t" \
    "ds_read_b128 v[216:219], v235 offset:28672\n\t" \
    "ds_read_b128 v[208:211], v231 offset:28672\n\t" \
    "s_waitcnt lgkmcnt(4)\n\t" \
    "v_mfma_f32_32x32x16_bf16 v[64:79], v[148:151], v[112:115], v[32:47]\n\t" \
    "v_exp_f32 v220, v80\n\t" \
    "v_exp_f32 v221, v81\n\t" \
    "v_exp_f32 v222, v82\n\t" \
    "v_mfma_f32_32x32x16_bf16 v[164:179], v[204:207], v[196:199], v[164:179]\n\t" \
    "v_exp_f32 v223, v83\n\t" \
    "v_exp_f32 v224, v84\n\t" \
    "v_cvt_pk_bf16_f32 v104, v220, v221\n\t" \
    "ds_read_b128 v[212:215], v231 offset:32768\n\t" \
    "v_mfma_f32_32x32x16_bf16 v[64:79], v[152:155], v[116:119], v[64:79]\n\t" \
    "v_exp_f32 v225, v85\n\t" \
    "v_exp_f32 v226, v86\n\t" \
    "v_cvt_pk_bf16_f32 v105, v222, v223\n\t" \
    "v_mfma_f32_32x32x16_bf16 v[180:195], v[204:207], v[200:203], v[180:195]\n\t" \
    "v_exp_f32 v227, v87\n\t" \
    "v_cvt_pk_bf16_f32 v106, v224, v225\n\t" \
    "v_cvt_pk_bf16_f32 v107, v226, v227\n\t" \
    "s_waitcnt lgkmcnt(0)\n\t" \
    "s_barrier\n\t" \
    "v_mfma_f32_16x16x32_bf16 v[128:131], v[132:135], v[100:103], v[128:131]\n\t" \
    "ds_read_b128 v[204:207], v242 offset:0\n\t" \
    "ds_read_b128 v[196:199], v238 offset:0\n\t" \
    "v_mfma_f32_32x32x16_bf16 v[0:15], v[156:159], v[96:99], v[0:15]\n\t" \
    "v_exp_f32 v220, v88\n\t" \
    "v_exp_f32 v221, v89\n\t" \
    "v_exp_f32 v222, v90\n\t" \
    "v_mfma_f32_32x32x16_bf16 v[164:179], v[216:219], v[208:211], v[164:179]\n\t" \
    "v_exp_f32 v223, v91\n\t" \
    "v_exp_f32 v224, v92\n\t" \
    "v_cvt_pk_bf16_f32 v108, v220, v221\n\t" \
    "ds_read_b128 v[200:203], v238 offset:4096\n\t" \
    "v_mfma_f32_32x32x16_bf16 v[0:15], v[160:163], v[100:103], v[0:15]\n\t" \
    "v_exp_f32 v225, v93\n\t" \
    "v_exp_f32 v226, v94\n\t" \
    "v_cvt_pk_bf16_f32 v109, v222, v223\n\t" \
    "v_mfma_f32_32x32x16_bf16 v[180:195], v[216:219], v[212:215], v[180:195]\n\t" \
    "v_exp_f32 v227, v95\n\t" \
    "v_cvt_pk_bf16_f32 v110, v224, v225\n\t" \
    "v_cvt_pk_bf16_f32 v111, v226, v227\n\t" \
    "v_mfma_f32_16x16x32_bf16 v[128:131], v[136:139], v[104:107], v[128:131]\n\t" \
    "ds_read_b128 v[216:219], v243 offset:0\n\t" \
    "ds_read_b128 v[208:211], v239 offset:0\n\t" \
    "s_waitcnt lgkmcnt(2)\n\t" \
    "v_mfma_f32_32x32x16_bf16 v[80:95], v[148:151], v[120:123], v[48:63]\n\t" \
    "v_exp_f32 v220, v64\n\t" \
    "v_exp_f32 v221, v65\n\t" \
    "v_exp_f32 v222, v66\n\t" \
    "v_mfma_f32_32x32x16_bf16 v[164:179], v[204:207], v[196:199], v[164:179]\n\t" \
    "v_exp_f32 v223, v67\n\t" \
    "v_exp_f32 v224, v68\n\t" \
    "v_cvt_pk_bf16_f32 v96, v220, v221\n\t" \
    "ds_read_b128 v[212:215], v239 offset:4096\n\t" \
    "v_mfma_f32_32x32x16_bf16 v[80:95], v[152:155], v[124:127], v[80:95]\n\t" \
    "v_exp_f32 v225, v69\n\t" \
    "v_exp_f32 v226, v70\n\t" \
    "v_cvt_pk_bf16_f32 v97, v222, v223\n\t" \
    "ds_read_b128 v[148:151], v237 offset:0\n\t" \
    "v_mfma_f32_32x32x16_bf16 v[180:195], v[204:207], v[200:203], v[180:195]\n\t" \
    "v_exp_f32 v227, v71\n\t" \
    "v_cvt_pk_bf16_f32 v98, v224, v225\n\t" \
    "ds_read_b128 v[152:155], v237 offset:1024\n\t" \
    "v_cvt_pk_bf16_f32 v99, v226, v227\n\t" \
    "v_mfma_f32_16x16x32_bf16 v[128:131], v[136:139], v[108:111], v[128:131]\n\t" \
    "ds_read_b128 v[204:207], v244 offset:0\n\t" \
    "ds_read_b128 v[196:199], v240 offset:0\n\t" \
    "s_waitcnt lgkmcnt(4)\n\t" \
    "v_mfma_f32_32x32x16_bf16 v[16:31], v[156:159], v[104:107], v[16:31]\n\t" \
    "v_exp_f32 v220, v72\n\t" \
    "v_exp_f32 v221, v73\n\t" \
    "v_exp_f32 v222, v74\n\t" \
    "v_mfma_f32_32x32x16_bf16 v[164:179], v[216:219], v[208:211], v[164:179]\n\t" \
    "v_exp_f32 v223, v75\n\t" \
    "v_exp_f32 v224, v76\n\t" \
    "v_cvt_pk_bf16_f32 v100, v220, v221\n\t" \
    "ds_read_b128 v[200:203], v240 offset:4096\n\t" \
    "v_mfma_f32_32x32x16_bf16 v[16:31], v[160:163], v[108:111], v[16:31]\n\t" \
    "v_exp_f32 v225, v77\n\t" \
    "v_exp_f32 v226, v78\n\t" \
    "v_cvt_pk_bf16_f32 v101, v222, v223\n\t" \
    "ds_read_b128 v[156:159], v237 offset:2048\n\t" \
    "v_mfma_f32_32x32x16_bf16 v[180:195], v[216:219], v[212:215], v[180:195]\n\t" \
    "v_exp_f32 v227, v79\n\t" \
    "v_cvt_pk_bf16_f32 v102, v224, v225\n\t" \
    "ds_read_b128 v[160:163], v237 offset:3072\n\t" \
    "v_cvt_pk_bf16_f32 v103, v226, v227\n\t" \
    "v_mfma_f32_16x16x32_bf16 v[128:131], v[132:135], v[96:99], v[128:131]\n\t" \
    "ds_read_b128 v[216:219], v245 offset:0\n\t" \
    "ds_read_b128 v[208:211], v241 offset:0\n\t" \
    "s_waitcnt lgkmcnt(4)\n\t" \
    "v_mfma_f32_32x32x16_bf16 v[64:79], v[148:151], v[112:115], v[32:47]\n\t" \
    "v_exp_f32 v220, v80\n\t" \
    "v_exp_f32 v221, v81\n\t" \
    "v_exp_f32 v222, v82\n\t" \
    "v_mfma_f32_32x32x16_bf16 v[164:179], v[204:207], v[196:199], v[164:179]\n\t" \
    "v_exp_f32 v223, v83\n\t" \
    "v_exp_f32 v224, v84\n\t" \
    "v_cvt_pk_bf16_f32 v104, v220, v221\n\t" \
    "ds_read_b128 v[212:215], v241 offset:4096\n\t" \
    "v_mfma_f32_32x32x16_bf16 v[64:79], v[152:155], v[116:119], v[64:79]\n\t" \
    "v_exp_f32 v225, v85\n\t" \
    "v_exp_f32 v226, v86\n\t" \
    "v_cvt_pk_bf16_f32 v105, v222, v223\n\t" \
    "v_mfma_f32_32x32x16_bf16 v[180:195], v[204:207], v[200:203], v[180:195]\n\t" \
    "v_exp_f32 v227, v87\n\t" \
    "v_cvt_pk_bf16_f32 v106, v224, v225\n\t" \
    "v_cvt_pk_bf16_f32 v107, v226, v227\n\t" \
    "s_waitcnt lgkmcnt(0)\n\t" \
    "s_barrier\n\t" \
    "v_mfma_f32_16x16x32_bf16 v[128:131], v[132:135], v[100:103], v[128:131]\n\t" \
    "ds_read_b128 v[204:207], v242 offset:28672\n\t" \
    "ds_read_b128 v[196:199], v238 offset:28672\n\t" \
    "v_mfma_f32_32x32x16_bf16 v[0:15], v[156:159], v[96:99], v[0:15]\n\t" \
    "v_exp_f32 v220, v88\n\t" \
    "v_exp_f32 v221, v89\n\t" \
    "v_exp_f32 v222, v90\n\t" \
    "v_mfma_f32_32x32x16_bf16 v[164:179], v[216:219], v[208:211], v[164:179]\n\t" \
    "v_exp_f32 v223, v91\n\t" \
    "v_exp_f32 v224, v92\n\t" \
    "v_cvt_pk_bf16_f32 v108, v220, v221\n\t" \
    "ds_read_b128 v[200:203], v238 offset:32768\n\t" \
    "v_mfma_f32_32x32x16_bf16 v[0:15], v[160:163], v[100:103], v[0:15]\n\t" \
    "v_exp_f32 v225, v93\n\t" \
    "v_exp_f32 v226, v94\n\t" \
    "v_cvt_pk_bf16_f32 v109, v222, v223\n\t" \
    "v_mfma_f32_32x32x16_bf16 v[180:195], v[216:219], v[212:215], v[180:195]\n\t" \
    "v_exp_f32 v227, v95\n\t" \
    "v_cvt_pk_bf16_f32 v110, v224, v225\n\t" \
    "v_cvt_pk_bf16_f32 v111, v226, v227\n\t" \
    "v_mfma_f32_16x16x32_bf16 v[128:131], v[136:139], v[104:107], v[128:131]\n\t" \
    "ds_read_b128 v[216:219], v243 offset:28672\n\t" \
    "ds_read_b128 v[208:211], v239 offset:28672\n\t" \
    "s_waitcnt lgkmcnt(2)\n\t" \
    "v_mfma_f32_32x32x16_bf16 v[80:95], v[148:151], v[120:123], v[48:63]\n\t" \
    "v_exp_f32 v220, v64\n\t" \
    "v_exp_f32 v221, v65\n\t" \
    "v_exp_f32 v222, v66\n\t" \
    "v_mfma_f32_32x32x16_bf16 v[164:179], v[204:207], v[196:199], v[164:179]\n\t" \
    "v_exp_f32 v223, v67\n\t" \
    "v_exp_f32 v224, v68\n\t" \
    "v_cvt_pk_bf16_f32 v96, v220, v221\n\t" \
    "ds_read_b128 v[212:215], v239 offset:32768\n\t" \
    "v_mfma_f32_32x32x16_bf16 v[80:95], v[152:155], v[124:127], v[80:95]\n\t" \
    "v_exp_f32 v225, v69\n\t" \
    "v_exp_f32 v226, v70\n\t" \
    "v_cvt_pk_bf16_f32 v97, v222, v223\n\t" \
    "ds_read_b128 v[148:151], v237 offset:28672\n\t" \
    "v_mfma_f32_32x32x16_bf16 v[180:195], v[204:207], v[200:203], v[180:195]\n\t" \
    "v_exp_f32 v227, v71\n\t" \
    "v_cvt_pk_bf16_f32 v98, v224, v225\n\t" \
    "ds_read_b128 v[152:155], v237 offset:29696\n\t" \
    "v_cvt_pk_bf16_f32 v99, v226, v227\n\t" \
    "v_mfma_f32_16x16x32_bf16 v[128:131], v[136:139], v[108:111], v[128:131]\n\t" \
    "ds_read_b128 v[204:207], v244 offset:28672\n\t" \
    "ds_read_b128 v[196:199], v240 offset:28672\n\t" \
    "s_waitcnt lgkmcnt(4)\n\t" \
    "v_mfma_f32_32x32x16_bf16 v[16:31], v[156:159], v[104:107], v[16:31]\n\t" \
    "v_exp_f32 v220, v72\n\t" \
    "v_exp_f32 v221, v73\n\t" \
    "v_exp_f32 v222, v74\n\t" \
    "v_mfma_f32_32x32x16_bf16 v[164:179], v[216:219], v[208:211], v[164:179]\n\t" \
    "v_exp_f32 v223, v75\n\t" \
    "v_exp_f32 v224, v76\n\t" \
    "v_cvt_pk_bf16_f32 v100, v220, v221\n\t" \
    "ds_read_b128 v[200:203], v240 offset:32768\n\t" \
    "v_mfma_f32_32x32x16_bf16 v[16:31], v[160:163], v[108:111], v[16:31]\n\t" \
    "v_exp_f32 v225, v77\n\t" \
    "v_exp_f32 v226, v78\n\t" \
    "v_cvt_pk_bf16_f32 v101, v222, v223\n\t" \
    "ds_read_b128 v[156:159], v237 offset:30720\n\t" \
    "v_mfma_f32_32x32x16_bf16 v[180:195], v[216:219], v[212:215], v[180:195]\n\t" \
    "v_exp_f32 v227, v79\n\t" \
    "v_cvt_pk_bf16_f32 v102, v224, v225\n\t" \
    "ds_read_b128 v[160:163], v237 offset:31744\n\t" \
    "v_cvt_pk_bf16_f32 v103, v226, v227\n\t" \
    "v_mfma_f32_16x16x32_bf16 v[128:131], v[132:135], v[96:99], v[128:131]\n\t" \
    "ds_read_b128 v[216:219], v245 offset:28672\n\t" \
    "ds_read_b128 v[208:211], v241 offset:28672\n\t" \
    "s_waitcnt lgkmcnt(4)\n\t" \
    "v_mfma_f32_32x32x16_bf16 v[64:79], v[148:151], v[112:115], v[32:47]\n\t" \
    "v_exp_f32 v220, v80\n\t" \
    "v_exp_f32 v221, v81\n\t" \
    "v_exp_f32 v222, v82\n\t" \
    "v_mfma_f32_32x32x16_bf16 v[164:179], v[204:207], v[196:199], v[164:179]\n\t" \
    "v_exp_f32 v223, v83\n\t" \
    "v_exp_f32 v224, v84\n\t" \
    "v_cvt_pk_bf16_f32 v104, v220, v221\n\t" \
    "ds_read_b128 v[212:215], v241 offset:32768\n\t" \
    "v_mfma_f32_32x32x16_bf16 v[64:79], v[152:155], v[116:119], v[64:79]\n\t" \
    "v_exp_f32 v225, v85\n\t" \
    "v_exp_f32 v226, v86\n\t" \
    "v_cvt_pk_bf16_f32 v105, v222, v223\n\t" \
    "v_mfma_f32_32x32x16_bf16 v[180:195], v[204:207], v[200:203], v[180:195]\n\t" \
    "v_exp_f32 v227, v87\n\t" \
    "v_cvt_pk_bf16_f32 v106, v224, v225\n\t" \
    "v_cvt_pk_bf16_f32 v107, v226, v227\n\t" \
    "s_waitcnt lgkmcnt(0)\n\t" \
    "s_barrier\n\t" \
    "v_mfma_f32_16x16x32_bf16 v[128:131], v[132:135], v[100:103], v[128:131]\n\t" \
    "ds_read_b128 v[204:207], v232 offset:0\n\t" \
    "ds_read_b128 v[196:199], v228 offset:0\n\t" \
    "v_mfma_f32_32x32x16_bf16 v[0:15], v[156:159], v[96:99], v[0:15]\n\t" \
    "v_exp_f32 v220, v88\n\t" \
    "v_exp_f32 v221, v89\n\t" \
    "v_exp_f32 v222, v90\n\t" \
    "v_mfma_f32_32x32x16_bf16 v[164:179], v[216:219], v[208:211], v[164:179]\n\t" \
    "v_exp_f32 v223, v91\n\t" \
    "v_exp_f32 v224, v92\n\t" \
    "v_cvt_pk_bf16_f32 v108, v220, v221\n\t" \
    "ds_read_b128 v[200:203], v228 offset:4096\n\t" \
    "v_mfma_f32_32x32x16_bf16 v[0:15], v[160:163], v[100:103], v[0:15]\n\t" \
    "v_exp_f32 v225, v93\n\t" \
    "v_exp_f32 v226, v94\n\t" \
    "v_cvt_pk_bf16_f32 v109, v222, v223\n\t" \
    "v_mfma_f32_32x32x16_bf16 v[180:195], v[216:219], v[212:215], v[180:195]\n\t" \
    "v_exp_f32 v227, v95\n\t" \
    "v_cvt_pk_bf16_f32 v110, v224, v225\n\t" \
    "v_cvt_pk_bf16_f32 v111, v226, v227\n\t" \
    "v_add_f32 v144, v128, v129\n\t" \
    "v_cmp_nge_f32 vcc, 1.0, v144\n\t" \
    "s_cbranch_vccnz .Lslow0_%=\n\t" \
    ".Lback0_%=:\n\t" \
    "s_sub_u32 %[cnt], %[cnt], 1\n\t" \
    "s_cmp_lg_u32 %[cnt], 0\n\t" \
    "s_cbranch_scc1 .Lloop_%=\n\t" \
    ".subsection 1\n\t" \
    ".Lslow0_%=:\n\t" \
    "s_nop 15\n\t" \
    "s_nop 15\n\t" \
    "v_cmp_ngt_f32 vcc, %[big], v128\n\t" \
    "s_or_b64 %[ovf], %[ovf], vcc\n\t" \
    "v_frexp_exp_i32_f32 v144, v128\n\t" \
    "v_add_u32 v144, 79, v144\n\t" \
    "v_max_i32 v144, 0, v144\n\t" \
    "v_sub_u32 v145, 0, v144\n\t" \
    "v_cvt_f32_i32 v146, v144\n\t" \
    "v_ldexp_f32 v128, v128, v145\n\t" \
    "v_add_f32 v140, v140, v146\n\t" \
    "v_ldexp_f32 v0, v0, v145\n\t" \
    "v_ldexp_f32 v1, v1, v145\n\t" \
    "v_ldexp_f32 v2, v2, v145\n\t" \
    "v_ldexp_f32 v3, v3, v145\n\t" \
    "v_ldexp_f32 v4, v4, v145\n\t" \
    "v_ldexp_f32 v5, v5, v145\n\t" \
    "v_ldexp_f32 v6, v6, v145\n\t" \
    "v_ldexp_f32 v7, v7, v145\n\t" \
    "v_ldexp_f32 v8, v8, v145\n\t" \
    "v_ldexp_f32 v9, v9, v145\n\t" \
    "v_ldexp_f32 v10, v10, v145\n\t" \
    "v_ldexp_f32 v11, v11, v145\n\t" \
    "v_ldexp_f32 v12, v12, v145\n\t" \
    "v_ldexp_f32 v13, v13, v145\n\t" \
    "v_ldexp_f32 v14, v14, v145\n\t" \
    "v_ldexp_f32 v15, v15, v145\n\t" \
    "v_sub_f32 v32, v32, v146\n\t" \
    "v_sub_f32 v33, v33, v146\n\t" \
    "v_sub_f32 v34, v34, v146\n\t" \
    "v_sub_f32 v35, v35, v146\n\t" \
    "v_sub_f32 v36, v36, v146\n\t" \
    "v_sub_f32 v37, v37, v146\n\t" \
    "v_sub_f32 v38, v38, v146\n\t" \
    "v_sub_f32 v39, v39, v146\n\t" \
    "v_sub_f32 v40, v40, v146\n\t" \
    "v_sub_f32 v41, v41, v146\n\t" \
    "v_sub_f32 v42, v42, v146\n\t" \
    "v_sub_f32 v43, v43, v146\n\t" \
    "v_sub_f32 v44, v44, v146\n\t" \
    "v_sub_f32 v45, v45, v146\n\t" \
    "v_sub_f32 v46, v46, v146\n\t" \
    "v_sub_f32 v47, v47, v146\n\t" \
    "v_sub_f32 v64, v64, v146\n\t" \
    "v_sub_f32 v65, v65, v146\n\t" \
    "v_sub_f32 v66, v66, v146\n\t" \
    "v_sub_f32 v67, v67, v146\n\t" \
    "v_sub_f32 v68, v68, v146\n\t" \
    "v_sub_f32 v69, v69, v146\n\t" \
    "v_sub_f32 v70, v70, v146\n\t" \
    "v_sub_f32 v71, v71, v146\n\t" \
    "v_sub_f32 v72, v72, v146\n\t" \
    "v_sub_f32 v73, v73, v146\n\t" \
    "v_sub_f32 v74, v74, v146\n\t" \
    "v_sub_f32 v75, v75, v146\n\t" \
    "v_sub_f32 v76, v76, v146\n\t" \
    "v_sub_f32 v77, v77, v146\n\t" \
    "v_sub_f32 v78, v78, v146\n\t" \
    "v_sub_f32 v79, v79, v146\n\t" \
    "v_cmp_ngt_f32 vcc, %[big], v129\n\t" \
    "s_or_b64 %[ovf], %[ovf], vcc\n\t" \
    "v_frexp_exp_i32_f32 v144, v129\n\t" \
    "v_add_u32 v144, 79, v144\n\t" \
    "v_max_i32 v144, 0, v144\n\t" \
    "v_sub_u32 v145, 0, v144\n\t" \
    "v_cvt_f32_i32 v146, v144\n\t" \
    "v_ldexp_f32 v129, v129, v145\n\t" \
    "v_add_f32 v141, v141, v146\n\t" \
    "v_ldexp_f32 v16, v16, v145\n\t" \
    "v_ldexp_f32 v17, v17, v145\n\t" \
    "v_ldexp_f32 v18, v18, v145\n\t" \
    "v_ldexp_f32 v19, v19, v145\n\t" \
    "v_ldexp_f32 v20, v20, v145\n\t" \
    "v_ldexp_f32 v21, v21, v145\n\t" \
    "v_ldexp_f32 v22, v22, v145\n\t" \
    "v_ldexp_f32 v23, v23, v145\n\t" \
    "v_ldexp_f32 v24, v24, v145\n\t" \
    "v_ldexp_f32 v25, v25, v145\n\t" \
    "v_ldexp_f32 v26, v26, v145\n\t" \
    "v_ldexp_f32 v27, v27, v145\n\t" \
    "v_ldexp_f32 v28, v28, v145\n\t" \
    "v_ldexp_f32 v29, v29, v145\n\t" \
    "v_ldexp_f32 v30, v30, v145\n\t" \
    "v_ldexp_f32 v31, v31, v145\n\t" \
    "v_sub_f32 v48, v48, v146\n\t" \
    "v_sub_f32 v49, v49, v146\n\t" \
    "v_sub_f32 v50, v50, v146\n\t" \
    "v_sub_f32 v51, v51, v146\n\t" \
    "v_sub_f32 v52, v52, v146\n\t" \
    "v_sub_f32 v53, v53, v146\n\t" \
    "v_sub_f32 v54, v54, v146\n\t" \
    "v_sub_f32 v55, v55, v146\n\t" \
    "v_sub_f32 v56, v56, v146\n\t" \
    "v_sub_f32 v57, v57, v146\n\t" \
    "v_sub_f32 v58, v58, v146\n\t" \
    "v_sub_f32 v59, v59, v146\n\t" \
    "v_sub_f32 v60, v60, v146\n\t" \
    "v_sub_f32 v61, v61, v146\n\t" \
    "v_sub_f32 v62, v62, v146\n\t" \
    "v_sub_f32 v63, v63, v146\n\t" \
    "v_lshlrev_b32 v144, 16, v104\n\t" \
    "v_and_b32 v146, 0xffff0000, v104\n\t" \
    "v_ldexp_f32 v144, v144, v145\n\t" \
    "v_ldexp_f32 v146, v146, v145\n\t" \
    "v_cvt_pk_bf16_f32 v104, v144, v146\n\t" \
    "v_lshlrev_b32 v144, 16, v105\n\t" \
    "v_and_b32 v146, 0xffff0000, v105\n\t" \
    "v_ldexp_f32 v144, v144, v145\n\t" \
    "v_ldexp_f32 v146, v146, v145\n\t" \
    "v_cvt_pk_bf16_f32 v105, v144, v146\n\t" \
    "v_lshlrev_b32 v144, 16, v106\n\t" \
    "v_and_b32 v146, 0xffff0000, v106\n\t" \
    "v_ldexp_f32 v144, v144, v145\n\t" \
    "v_ldexp_f32 v146, v146, v145\n\t" \
    "v_cvt_pk_bf16_f32 v106, v144, v146\n\t" \
    "v_lshlrev_b32 v144, 16, v107\n\t" \
    "v_and_b32 v146, 0xffff0000, v107\n\t" \
    "v_ldexp_f32 v144, v144, v145\n\t" \
    "v_ldexp_f32 v146, v146, v145\n\t" \
    "v_cvt_pk_bf16_f32 v107, v144, v146\n\t" \
    "v_lshlrev_b32 v144, 16, v108\n\t" \
    "v_and_b32 v146, 0xffff0000, v108\n\t" \
    "v_ldexp_f32 v144, v144, v145\n\t" \
    "v_ldexp_f32 v146, v146, v145\n\t" \
    "v_cvt_pk_bf16_f32 v108, v144, v146\n\t" \
    "v_lshlrev_b32 v144, 16, v109\n\t" \
    "v_and_b32 v146, 0xffff0000, v109\n\t" \
    "v_ldexp_f32 v144, v144, v145\n\t" \
    "v_ldexp_f32 v146, v146, v145\n\t" \
    "v_cvt_pk_bf16_f32 v109, v144, v146\n\t" \
    "v_lshlrev_b32 v144, 16, v110\n\t" \
    "v_and_b32 v146, 0xffff0000, v110\n\t" \
    "v_ldexp_f32 v144, v144, v145\n\t" \
    "v_ldexp_f32 v146, v146, v145\n\t" \
    "v_cvt_pk_bf16_f32 v110, v144, v146\n\t" \
    "v_lshlrev_b32 v144, 16, v111\n\t" \
    "v_and_b32 v146, 0xffff0000, v111\n\t" \
    "v_ldexp_f32 v144, v144, v145\n\t" \
    "v_ldexp_f32 v146, v146, v145\n\t" \
    "v_cvt_pk_bf16_f32 v111, v144, v146\n\t" \
    "s_nop 3\n\t" \
    "s_branch .Lback0_%=\n\t" \
    ".subsection 0\n\t" \
    "s_nop 15\n\t" \
    "s_nop 15\n\t"

constexpr int WO_AT = 24;
constexpr int F_SLOT = 28672, F_NST = 4, F_STG = F_SLOT * F_NST, F_LDS = F_STG + 4 * 4096;
constexpr int F_GN = 16384, F_KV = 24576;
struct FusedArgs { const bf16_t *cr, *ci; char* ws; bf16_t* out; const bf16_t* wo; };

__global__ __launch_bounds__(512, 2) void fused_kernel(FusedArgs a) {
    extern __shared__ __attribute__((aligned(16))) char smem[];
    LAS char* lds = (LAS char*)smem;
    const unsigned lds0 = (unsigned)(uintptr_t)lds;
    const int tid = TIDX, lane = tid & 63;
    const int wid = __builtin_amdgcn_readfirstlane(tid >> 6);
    const int b = BIDX;
    const int xcd = b & 7, jj = b >> 3;
    const int mp = (xcd & 3) * 4 + (jj & 3), np = (xcd >> 2) * 8 + (jj >> 2);
    const char* ws = a.ws;
    const srd_t srdK = mksrd(ws + WS_KF, 128u << 10), srdV = mksrd(ws + WS_VF, 128u << 10);
    if (wid >= 4) {
        const int pw = wid - 4;
        const srd_t scr = mksrd(a.cr + (size_t)mp * 128 * SEQ, 128 * SEQ * 2), sci = mksrd(a.ci + (size_t)mp * 128 * SEQ, 128 * SEQ * 2);
        const srd_t sab = mksrd(ws + WS_ABT + (size_t)np * 64 * 4096 * 2, 64 * 4096 * 2);
        unsigned voffM[4], voffN[2];
#pragma unroll
        for (int i = 0; i < 4; ++i) { const int row = (pw * 4 + i) * 8 + (lane >> 3), ch = (lane & 7) ^ ((row >> 1) & 7); voffM[i] = row * (SEQ * 2) + ch * 16 - i * 1024; }
#pragma unroll
        for (int i = 0; i < 2; ++i) { const int row = (pw * 2 + i) * 8 + (lane >> 3), ch = (lane & 7) ^ ((row >> 1) & 7); voffN[i] = row * 8192 + ch * 16 - i * 1024; }
        const unsigned vkv = lane * 16 + (pw & 1) * 1024;
        const srd_t skv = pw < 2 ? srdK : srdV;
        const unsigned kvo = pw < 2 ? 2048u : 0u;
        auto issue = [&](int i, unsigned slot) {
            const unsigned st = lds0 + slot;
            const bool in = i < 64;
            dma4(st + pw * 4096, i < 32 ? scr : sci, in ? (i & 31) * 128 : 0x40000000u, voffM[0], voffM[1], voffM[2], voffM[3]);
            dma2(st + F_GN + pw * 2048, sab, in ? i * 128 : 0x40000000u, voffN[0], voffN[1]);
            dma1(st + F_KV + pw * 1024, skv, in ? i * 2048 + kvo : 0x40000000u, vkv);
        };
#pragma unroll
        for (int s = 0; s < F_NST; ++s) issue(s, s * F_SLOT);
        wait_bar<(F_NST - 1) * 7>();
        const srd_t swo = mksrd(a.wo, DM * DM * 2), sop = mksrd(ws + WS_OP + (size_t)mp * 128 * DM * 2, 128 * DM * 2);
        unsigned voffW[2], voffO[4];
#pragma unroll
        for (int i = 0; i < 2; ++i) { const int k = 8 * (2 * pw + i) + (lane >> 3), ch = (lane & 7) ^ (((k >> 1) & 1) << 2), kk = (k & ~12) | ((k & 4) << 1) | ((k & 8) >> 1);
                                      voffW[i] = kk * (DM * 2) + ch * 16 - i * 1024; }
#pragma unroll
        for (int i = 0; i < 4; ++i) { const int row = (pw * 4 + i) * 8 + (lane >> 3), ch = (lane & 7) ^ ((row >> 1) & 7); voffO[i] = row * (DM * 2) + ch * 16 - i * 1024; }
        auto issueW = [&](int kt, unsigned slot) { dma2(lds0 + slot + F_GN + pw * 2048, swo, kt < 16 ? kt * 64 * DM * 2 + np * 128 : 0x40000000u, voffW[0], voffW[1]); };
        auto issueO = [&](int kt, unsigned slot) { dma4_sc1(lds0 + slot + pw * 4096, sop, kt < 16 ? kt * 128 : 0x40000000u, voffO[0], voffO[1], voffO[2], voffO[3]); };
        unsigned st = 0, sink = 0;
#pragma unroll 1
        for (int i = 0; i < 60; ++i) {
            wait_bar<(F_NST - 2) * 7 + 0>();
            issue(i + F_NST, st);
            if (i == WO_AT && pw == 3) touch(sink, swo, lane * 128, b * 8192);
            st = (st + F_SLOT == F_NST * F_SLOT) ? 0 : st + F_SLOT;
        }
        wait_bar<14>(); issueW(0, 0 * F_SLOT);
        wait_bar<9>();  issueW(1, 1 * F_SLOT);
        wait_bar<4>();  issueW(2, 2 * F_SLOT);
        wait_bar<6>();  issueW(3, 3 * F_SLOT);
        wait_bar<8>();
        {
            const GAS unsigned* cnt = (const GAS unsigned*)(ws + WS_FLAG) + (mp >> 1) * 64;
            unsigned spins = 0;
            while (__builtin_amdgcn_readfirstlane(__hip_atomic_load(cnt, __ATOMIC_RELAXED, __HIP_MEMORY_SCOPE_AGENT)) < 32u) {
                __builtin_amdgcn_s_sleep(2);
                if (++spins > (1u << 22)) break;
            }
        }
#pragma unroll
        for (int kt = 0; kt < 4; ++kt) issueO(kt, kt * F_SLOT);
        wait_bar<12>();
        wait_bar<8>();  issueO(4, 0 * F_SLOT); issueW(4, 0 * F_SLOT);
        wait_bar<10>(); issueO(5, 1 * F_SLOT); issueW(5, 1 * F_SLOT);
        st = 2 * F_SLOT;
#pragma unroll 1
        for (int kt = 2; kt < 16; ++kt) {
            wait_bar<12>();
            issueO(kt + 4, st); issueW(kt + 4, st);
            st = (st + F_SLOT == F_NST * F_SLOT) ? 0 : st + F_SLOT;
        }
        wait_bar<0>();
        touch_done(sink);
        return;
    }
    const int h = lane >> 5, l31 = lane & 31;
    const int head = b & 31, qb = b >> 5, qt0 = qb * 8 + wid * 2;
    const srd_t srdQ = mksrd(ws + WS_QF, 4u << 20);
    const unsigned vo = lane * 16, vo2 = lane * 16 + 1024;
    bf16x8 q[2][2];
#pragma unroll
    for (int qt = 0; qt < 2; ++qt)
#pragma unroll
        for (int s = 0; s < 2; ++s) q[qt][s] = bld(srdQ, vo + s * 1024, (head * 64 + qt0 + qt) * 2048);
    bf16x8 Kr[2], Vr[2];
    Kr[0] = bld(srdK, vo, 0); Kr[1] = bld(srdK, vo2, 0);
    Vr[0] = bf16x8{}; Vr[1] = bf16x8{};
    bf16x8 ones[2];
    {
        const bool sel = (((lane >> 2) & 1) == ((lane >> 4) & 1));
#pragma unroll
        for (int qt = 0; qt < 2; ++qt) { const short v = (sel && (lane & 3) == qt) ? (short)0x3F80 : (short)0;
#pragma unroll
            for (int j = 0; j < 8; ++j) ones[qt][j] = v; }
    }
    f32x16 negm[2], O[2] = {f32x16{}, f32x16{}}, S[2], G[2] = {f32x16{}, f32x16{}};
    float mref[2];
    f32x4 l = {0.f, 0.f, 0.f, 0.f};
    u32x4 P[4] = {u32x4{}, u32x4{}, u32x4{}, u32x4{}};
    unsigned long long ovf = 0;
    auto rowmax = [&](const f32x16& x) {
        float mx = x[0];
#pragma unroll
        for (int r = 1; r < 16; ++r) mx = __builtin_fmaxf(mx, x[r]);
        auto rr = __builtin_amdgcn_permlane32_swap(__float_as_uint(mx), __float_as_uint(mx), false, false);
        return __builtin_fmaxf(__uint_as_float(rr[0]), __uint_as_float(rr[1]));
    };
    constexpr int NREF = 4;
    {
        float mx[2] = {-__builtin_inff(), -__builtin_inff()};
#pragma unroll
        for (int t = NREF - 1; t >= 0; --t) {
            bf16x8 k0 = Kr[0], k1 = Kr[1];
            if (t) { k0 = bld(srdK, vo, t * 2048); k1 = bld(srdK, vo2, t * 2048); }
#pragma unroll
            for (int qt = 0; qt < 2; ++qt) {
                f32x16 sr = MFMA32(k0, q[qt][0], f32x16{}); sr = MFMA32(k1, q[qt][1], sr);
                float m = sr[0];
#pragma unroll
                for (int r = 1; r < 16; ++r) m = __builtin_fmaxf(m, sr[r]);
                mx[qt] = __builtin_fmaxf(mx[qt], m);
                if (t == 0 && qt == 0) S[0] = sr;
            }
        }
#pragma unroll
        for (int qt = 0; qt < 2; ++qt) {
            auto rr = __builtin_amdgcn_permlane32_swap(__float_as_uint(mx[qt]), __float_as_uint(mx[qt]), false, false);
            const float mnew = __builtin_fmaxf(__uint_as_float(rr[0]), __uint_as_float(rr[1])) + REF_OFF;
            mref[qt] = mnew;
#pragma unroll
            for (int r = 0; r < 16; ++r) { negm[qt][r] = -mnew; if (qt == 0) S[0][r] -= mnew; }
        }
    }
    const int wm = wid >> 1, wn = wid & 1, sw = (lane >> 1) & 7;
    unsigned mo[4], no[4];
#pragma unroll
    for (int ks = 0; ks < 4; ++ks) { mo[ks] = (wm * 64 + l31) * 128 + (((2 * ks + h) ^ sw) << 4); no[ks] = F_GN + (wn * 32 + l31) * 128 + (((2 * ks + h) ^ sw) << 4); }
    const unsigned kvo = F_KV + lane * 16;
    bf16x8 GA[3];
    unsigned moB[4], noB[4];
#pragma unroll
    for (int ks = 0; ks < 4; ++ks) { moB[ks] = lds0 + mo[ks] + 2 * F_SLOT; noB[ks] = lds0 + no[ks] + 2 * F_SLOT; mo[ks] += lds0; no[ks] += lds0; }
    unsigned kvA = lds0 + kvo, kvB = lds0 + kvo + 2 * F_SLOT;
    wait_bar<0>();
    GA[0] = *(const LAS bf16x8*)(uintptr_t)mo[0]; GA[1] = *(const LAS bf16x8*)(uintptr_t)(mo[0] + 4096); GA[2] = *(const LAS bf16x8*)(uintptr_t)no[0];
    const float big = 1.329228e36f;
    u32x4 moAv = {mo[0], mo[1], mo[2], mo[3]}, noAv = {no[0], no[1], no[2], no[3]}, moBv = {moB[0], moB[1], moB[2], moB[3]}, noBv = {noB[0], noB[1], noB[2], noB[3]};
    {
        unsigned cnt;
        asm volatile("s_waitcnt lgkmcnt(0)\n\ts_nop 7\n\t" FUSED_LOOP_ASM
            : "+{v[0:15]}"(O[0]), "+{v[16:31]}"(O[1]), "+{v[32:47]}"(negm[0]), "+{v[48:63]}"(negm[1]), "+{v[64:79]}"(S[0]), "=&{v[80:95]}"(S[1]),
              "+{v[96:99]}"(P[0]), "+{v[100:103]}"(P[1]), "+{v[104:107]}"(P[2]), "+{v[108:111]}"(P[3]), "+{v[128:131]}"(l), "+{v140}"(mref[0]), "+{v141}"(mref[1]),
              "+{v[148:151]}"(Kr[0]), "+{v[152:155]}"(Kr[1]), "+{v[156:159]}"(Vr[0]), "+{v[160:163]}"(Vr[1]), "+{v[164:179]}"(G[0]), "+{v[180:195]}"(G[1]),
              "+{v[196:199]}"(GA[0]), "+{v[200:203]}"(GA[1]), "+{v[204:207]}"(GA[2]), [ovf] "+s"(ovf), [cnt] "=&s"(cnt)
            : "{v[112:115]}"(q[0][0]), "{v[116:119]}"(q[0][1]), "{v[120:123]}"(q[1][0]), "{v[124:127]}"(q[1][1]), "{v[132:135]}"(ones[0]), "{v[136:139]}"(ones[1]),
              "{v[228:231]}"(moAv), "{v[232:235]}"(noAv), "{v236}"(kvA), "{v[238:241]}"(moBv), "{v[242:245]}"(noBv), "{v237}"(kvB), [big] "s"(big)
            : "vcc", "scc", "memory", "v144", "v145", "v146", "v208", "v209", "v210", "v211", "v212", "v213", "v214", "v215", "v216", "v217", "v218", "v219",
              "v220", "v221", "v222", "v223", "v224", "v225", "v226", "v227");
    }
    l = MFMA16(ones[1], BC8(P[2]), l); l = MFMA16(ones[1], BC8(P[3]), l);
    O[1] = MFMA32(Vr[0], BC8(P[2]), O[1]); O[1] = MFMA32(Vr[1], BC8(P[3]), O[1]);
    {
        float chk = l[0] + l[1];
#pragma unroll
        for (int r = 0; r < 16; ++r) chk += (O[0][r] - O[0][r]) + (O[1][r] - O[1][r]);
        const bool bad = (ovf != 0ull) || __builtin_amdgcn_ballot_w64(!(chk < big)) != 0ull;
        if (__builtin_expect(bad, 0)) {
#pragma unroll
            for (int qt = 0; qt < 2; ++qt) {
                f32x16 o = f32x16{}; float m = -__builtin_inff(), ls = 0.f;
#pragma unroll 1
                for (int t = 0; t < 64; ++t) {
                    const bf16x8 k0 = bld(srdK, vo, t * 2048), k1 = bld(srdK, vo2, t * 2048), v0 = bld(srdV, vo, t * 2048), v1 = bld(srdV, vo2, t * 2048);
                    f32x16 sr = MFMA32(k0, q[qt][0], f32x16{}); sr = MFMA32(k1, q[qt][1], sr);
                    const float mn = __builtin_fmaxf(m, rowmax(sr));
                    const float f = (mn == m) ? 1.f : EX(m - mn);
                    m = mn;
                    u32x4 p0, p1; float acc = 0.f;
#pragma unroll
                    for (int j = 0; j < 4; ++j) {
                        p0[j] = cvtpk(EX(sr[2 * j] - m), EX(sr[2 * j + 1] - m)); p1[j] = cvtpk(EX(sr[8 + 2 * j] - m), EX(sr[8 + 2 * j + 1] - m));
                        acc += __uint_as_float(p0[j] << 16) + __uint_as_float(p0[j] & 0xffff0000u) + __uint_as_float(p1[j] << 16) + __uint_as_float(p1[j] & 0xffff0000u);
                    }
                    { auto rr = __builtin_amdgcn_permlane32_swap(__float_as_uint(acc), __float_as_uint(acc), false, false); acc = __uint_as_float(rr[0]) + __uint_as_float(rr[1]); }
                    ls = ls * f + acc;
#pragma unroll
                    for (int r = 0; r < 16; ++r) o[r] *= f;
                    o = MFMA32(v0, BC8(p0), o); o = MFMA32(v1, BC8(p1), o);
                }
                O[qt] = o; l[qt] = ls;
            }
        }
    }
#pragma unroll
    for (int qt = 0; qt < 2; ++qt) {
        const float inv = __builtin_amdgcn_rcpf(l[qt]);
        f32x16 r;
#pragma unroll
        for (int i = 0; i < 16; ++i) r[i] = O[qt][i] * inv;
        bf16_t* op = (bf16_t*)(a.ws + WS_OP) + (size_t)((qt0 + qt) * 32 + l31) * DM + head * 32 + 8 * h;
#pragma unroll
        for (int s = 0; s < 2; ++s) st16_sc1(op + 16 * s, pack8(r, s, 1.f));
    }
    wait_bar<0>();
    if (wid == 0 && lane == 0) __hip_atomic_fetch_add((GAS unsigned*)(a.ws + WS_FLAG) + qb * 64, 1u, __ATOMIC_RELAXED, __HIP_MEMORY_SCOPE_AGENT);
    {
        f32x16 (&acc)[2][1] = *(f32x16 (*)[2][1])&G;
        store_rowmajor<2, 1>(acc, a.out + (size_t)(mp * 128 + wm * 64) * 2048 + 1024 + np * 64 + wn * 32, 2048, 1.f, lane, lds + F_STG + wid * 4096);
    }
    gemm_cons<2, 2, 2, 1, F_NST, 1, F_SLOT>(lds, 16, wid, lane, [&](f32x16 (&acc)[2][1], int wm2, int wn2) {
        store_rowmajor<2, 1>(acc, a.out + (size_t)(mp * 128 + wm2 * 64) * 2048 + np * 64 + wn2 * 32, 2048, 1.f, lane, lds + F_STG + wid * 4096); });
}

extern "C" void kernel_launch(void* const* d_in, const int* in_sizes, int n_in, void* d_out, int out_size, void* d_ws, size_t ws_size, hipStream_t stream) {
    const bf16_t* xa = (const bf16_t*)d_in[0]; const bf16_t* wq = (const bf16_t*)d_in[1]; const bf16_t* wk = (const bf16_t*)d_in[2];
    const bf16_t* wv = (const bf16_t*)d_in[3]; const bf16_t* wo = (const bf16_t*)d_in[4]; const bf16_t* xb = (const bf16_t*)d_in[5];
    const bf16_t* cr = (const bf16_t*)d_in[6]; const bf16_t* ci = (const bf16_t*)d_in[7]; const bf16_t* dr = (const bf16_t*)d_in[8]; const bf16_t* di = (const bf16_t*)d_in[9];
    char* ws = (char*)d_ws;
    hipFuncSetAttribute((const void*)proj_kernel, hipFuncAttributeMaxDynamicSharedMemorySize, P_LDS);
    ProjArgs pa{xa, wq, wk, wv, xb, dr, di, ws};
    proj_kernel<<<201, 768, P_LDS, stream>>>(pa);
    hipFuncSetAttribute((const void*)fused_kernel, hipFuncAttributeMaxDynamicSharedMemorySize, F_LDS);
    FusedArgs fa{cr, ci, ws, (bf16_t*)d_out, wo};
    fused_kernel<<<256, 512, F_LDS, stream>>>(fa);
}

#if defined(__HIP_DEVICE_COMPILE__)
#pragma clang attribute pop
#endif
```
